```python
import torch

def _softmax(x, axis=-1):
    return torch.softmax(x, dim=axis)
def _gelu(x):
    return torch.nn.functional.gelu(x, approximate="tanh")
def _silu(x):
    return torch.nn.functional.silu(x)
def _sigmoid(x):
    return torch.sigmoid(x)
def _layernorm(x, axis=-1, eps=1e-5):
    m = x.mean(dim=axis, keepdim=True)
    v = x.var(dim=axis, keepdim=True, unbiased=False)
    return (x - m) / torch.sqrt(v + eps)
def _scatter_add(tgt, idx, src, axis=0):
    return tgt.clone().index_add_(axis, idx, src)
def _segment_sum(src, seg, n):
    out = torch.zeros((n,) + src.shape[1:], dtype=src.dtype)
    return out.index_add_(0, seg, src)
def _pad(x, pw):
    flat = tuple(v for pair in reversed(pw) for v in pair)
    return torch.nn.functional.pad(x, flat)

kernel_name = 'standalone__linear_kernel_performer__prompt'

def setup_inputs():

    _w = (lambda a: (lambda t: t.to(torch.bfloat16) if t.is_floating_point() else t)(torch.as_tensor(a)))
    B, H, S, D = 1, 32, 2048, 192
    M = D
    return {
        "q": _w((lambda *s: torch.randn(*s, dtype=torch.bfloat16) * 0.05)(B, H, S, D)),
        "k": _w((lambda *s: torch.randn(*s, dtype=torch.bfloat16) * 1.6)(B, H, S, D)),
        "v": _w((lambda *s: torch.randn(*s, dtype=torch.bfloat16) * 1.6)(B, H, S, D)),
        "proj": _w((lambda *s: torch.randn(*s, dtype=torch.bfloat16) * 1.6)(M, D)),
    }

def reference(q, k, v, proj):
    B, H, S, D = q.shape
    M = proj.shape[0]
    scale = D ** -0.25
    qs = q * scale
    ks = k * scale
    qw = torch.einsum("bhsd,md->bhsm", qs, proj)
    kw = torch.einsum("bhsd,md->bhsm", ks, proj)
    qn = torch.einsum("bhsd,bhsd->bhs", qs, qs)[..., None] * 0.5
    kn = torch.einsum("bhsd,bhsd->bhs", ks, ks)[..., None] * 0.5
    inv_sqrt_m = M ** -0.5
    phi_q = torch.exp(qw - qn) * inv_sqrt_m
    phi_k = torch.exp(kw - kn) * inv_sqrt_m
    kv = torch.einsum("bhsm,bhsd->bhmd", phi_k, v)
    ksum = torch.einsum("bhsm->bhm", phi_k)
    num = torch.einsum("bhsm,bhmd->bhsd", phi_q, kv)
    den = torch.einsum("bhsm,bhm->bhs", phi_q, ksum)[..., None] + 1e-6
    return num / den
```

```cpp
#include <hip/hip_runtime.h>
#include <hip/hip_bf16.h>
#include <stdint.h>

typedef unsigned long long u64;
typedef __attribute__((ext_vector_type(8))) __bf16 bf16x8;
typedef __attribute__((ext_vector_type(2))) __bf16 bf16x2;
typedef __attribute__((ext_vector_type(2))) float f32x2;
typedef __attribute__((ext_vector_type(16))) float f32x16;
typedef __attribute__((ext_vector_type(4))) unsigned u32x4;
typedef __attribute__((ext_vector_type(2))) unsigned u32x2;
typedef __attribute__((ext_vector_type(4))) short s16x4;
#define LAS __attribute__((address_space(3)))
typedef LAS char* lptr;

#define NT_IN 0
#define NT_OUT 0
#define TS 0

constexpr int ROWB = 384;
constexpr int TILE = 12288;
constexpr int L_K = 0, L_V = 73728, L_X = 135168, L_BIAS = 147456, L_TAB = 147712, L_CNT = 148480, L_NON = 148488, L_END = 148512;
constexpr size_t W_P1 = 0;
constexpr size_t W_FIN = 32ull * 2 * 4 * 3 * 12 * 1024;
constexpr size_t W_KS = W_FIN + 32ull * 72 * 1024;
constexpr size_t W_INB = W_KS + 32ull * 8 * 3 * 128;
constexpr size_t W_HEL = W_INB + 256ull * 3 * 128;
constexpr size_t W_TS = W_HEL + 4096;
constexpr unsigned SPIN_MAX = 1u << 22;

__device__ __forceinline__ f32x16 mfma(u32x4 a, u32x4 b, f32x16 c) {
  return __builtin_amdgcn_mfma_f32_32x32x16_bf16(__builtin_bit_cast(bf16x8, a), __builtin_bit_cast(bf16x8, b), c, 0, 0, 0);
}
__device__ __forceinline__ unsigned pk(float a, float b) {
  f32x2 v = {a, b};
  return __builtin_bit_cast(unsigned, __builtin_convertvector(v, bf16x2));
}
__device__ __forceinline__ float dot2(unsigned a, unsigned b, float c) {
  return __builtin_amdgcn_fdot2_f32_bf16(__builtin_bit_cast(bf16x2, a), __builtin_bit_cast(bf16x2, b), c, false);
}
__device__ __forceinline__ u32x4 ld128(lptr p) { return *(LAS u32x4*)p; }
__device__ __forceinline__ u32x2 ldtr(lptr p) { return __builtin_bit_cast(u32x2, __builtin_amdgcn_ds_read_tr16_b64_v4i16((LAS s16x4*)p)); }
__device__ __forceinline__ float bflo(unsigned x) { return __uint_as_float(x << 16); }
__device__ __forceinline__ float bfhi(unsigned x) { return __uint_as_float(x & 0xffff0000u); }
__device__ __forceinline__ float halfsum(float x) {
  auto r = __builtin_amdgcn_permlane32_swap(__float_as_uint(x), __float_as_uint(x), false, false);
  return __uint_as_float(r[0]) + __uint_as_float(r[1]);
}
template <int NT>
__device__ __forceinline__ void dma(unsigned ldsaddr, unsigned voff, __amdgpu_buffer_rsrc_t srd, unsigned soff) {
  if constexpr (NT)
    asm volatile("s_nop 4\n\ts_mov_b32 m0, %0\n\ts_nop 0\n\tbuffer_load_dwordx4 %1, %2, %3 offen nt lds" ::"s"(ldsaddr), "v"(voff), "s"(srd), "s"(soff) : "memory");
  else
    asm volatile("s_nop 4\n\ts_mov_b32 m0, %0\n\ts_nop 0\n\tbuffer_load_dwordx4 %1, %2, %3 offen lds" ::"s"(ldsaddr), "v"(voff), "s"(srd), "s"(soff) : "memory");
}
#define WAITV(n) asm volatile("s_waitcnt vmcnt(" #n ")" ::: "memory")
#define WAITL() asm volatile("s_waitcnt lgkmcnt(0)" ::: "memory")
#define BAR() do { asm volatile("s_waitcnt lgkmcnt(0)" ::: "memory"); __builtin_amdgcn_s_barrier(); asm volatile("" ::: "memory"); } while (0)
typedef __attribute__((address_space(1))) u64 gu64;
__device__ __forceinline__ u64 ald(const char* p) { return __hip_atomic_load((gu64*)p, __ATOMIC_RELAXED, __HIP_MEMORY_SCOPE_AGENT); }
__device__ __forceinline__ void ast(char* p, u64 v) { __hip_atomic_store((gu64*)p, v, __ATOMIC_RELAXED, __HIP_MEMORY_SCOPE_AGENT); }
#define STAMP(i)

__device__ __forceinline__ void feat(const f32x16& acc, float c1, float nb, unsigned* o) {
#pragma unroll
  for (int i = 0; i < 8; ++i)
    o[i] = pk(__builtin_amdgcn_exp2f(__builtin_fmaf(acc[2 * i], c1, nb)), __builtin_amdgcn_exp2f(__builtin_fmaf(acc[2 * i + 1], c1, nb)));
}
__device__ __forceinline__ float rownorm(const u32x4* f) {
  float s0 = 0.f, s1 = 0.f;
#pragma unroll
  for (int i = 0; i < 12; ++i) {
    s0 = dot2(f[i][0], f[i][0], s0); s1 = dot2(f[i][1], f[i][1], s1);
    s0 = dot2(f[i][2], f[i][2], s0); s1 = dot2(f[i][3], f[i][3], s1);
  }
  return halfsum(s0 + s1);
}

__global__ __launch_bounds__(512, 2) void performer_fused(const char* __restrict__ q, const char* __restrict__ k, const char* __restrict__ v,
                                                          const char* __restrict__ proj, char* __restrict__ out, char* __restrict__ ws) {
  __shared__ __attribute__((aligned(16))) char lds_raw[L_END];
  lptr L = (lptr)lds_raw;
  const unsigned lbase = (unsigned)(uintptr_t)L;
  const int b = blockIdx.x;
  const int l = threadIdx.x & 63;
  const int w = __builtin_amdgcn_readfirstlane(threadIdx.x >> 6);
  const int head = (b & 7) * 4 + (b >> 6), i8 = (b >> 3) & 7, mh = i8 & 1, sc = i8 >> 1;
  const int h = l >> 5, r32 = l & 31;
  const float scale = 0.26864248f;
  const float LOG2E = 1.44269504089f;
  const float c1 = scale * LOG2E, cn = -0.5f * scale * scale * LOG2E, c0 = -3.79248125036f;
  const size_t hoff = (size_t)head * 2048 * ROWB;
  STAMP(0);

  unsigned fa[4];
#pragma unroll
  for (int kk = 0; kk < 4; ++kk) fa[kk] = r32 * ROWB + ((((2 * kk + h) ^ ((r32 >> 1) & 7))) << 4);
  auto ldfrags = [&](u32x4* f, unsigned base) {
#pragma unroll
    for (int ks = 0; ks < 12; ++ks) f[ks] = ld128(L + base + fa[ks & 3] + (ks >> 2) * 128);
  };

  if (w < 4) {
    unsigned vo[3];
#pragma unroll
    for (int p = 0; p < 3; ++p) { int Lc = p * 64 + l, row = Lc / 24, c = Lc % 24; vo[p] = row * ROWB + ((c ^ (row >> 1)) << 4); }
    const unsigned par = (w & 1) << 6;
    const __amdgpu_buffer_rsrc_t sK = __builtin_amdgcn_make_buffer_rsrc((void*)(k + hoff + (size_t)sc * 512 * ROWB), 0, 512 * ROWB, 0x00020000);
    const __amdgpu_buffer_rsrc_t sP = __builtin_amdgcn_make_buffer_rsrc((void*)proj, 0, 192 * ROWB, 0x00020000);
    const __amdgpu_buffer_rsrc_t sQ = __builtin_amdgcn_make_buffer_rsrc((void*)(q + hoff + (size_t)i8 * 256 * ROWB), 0, 256 * ROWB, 0x00020000);
    auto issueK = [&](int j, int slot) {
      const unsigned dst = lbase + L_K + slot * TILE + w * 3072;
      if (j < 16) {
#pragma unroll
        for (int p = 0; p < 3; ++p) dma<NT_IN>(dst + p * 1024, vo[p] ^ par, sK, j * TILE + w * 3072);
      } else {
#pragma unroll
        for (int p = 0; p < 3; ++p) dma<0>(dst + p * 1024, vo[p] ^ par, sP, (j - 16) * TILE + w * 3072);
      }
    };
    auto issueQ = [&](int t) {
      const unsigned dst = lbase + L_V + (1 + w) * TILE;
#pragma unroll
      for (int a = 0; a < 4; ++a)
#pragma unroll
        for (int p = 0; p < 3; ++p) dma<NT_IN>(dst + a * 3072 + p * 1024, vo[p] ^ ((a & 1) << 6), sQ, (2 * w + t) * TILE + a * 3072);
    };
#pragma unroll
    for (int t = 0; t < 3; ++t) issueK(16 + 3 * mh + t, 3 + t);
#pragma unroll
    for (int t = 0; t < 3; ++t) issueK(t, t);
    WAITV(6);
    BAR();
    u32x4 pf[12], kf[12];
    int islot = 0;
    int nslot = 1;
    auto round_open = [&](int r) {
      if (r == 0) { issueK(3, 3); issueK(4, 4); issueK(5, 5); }
      if (r <= 15) issueK(r + 6, islot);
      if (r == 13 + w) issueQ(0);
      islot = islot == 5 ? 0 : islot + 1;
    };
    auto round_close = [&](int r) {
      nslot = nslot == 5 ? 0 : nslot + 1;
      if (r == 13 && w == 0) WAITV(24);
      else if (r <= 13) WAITV(12);
      else if (r == 16) { if (w < 2) WAITV(0); else WAITV(12); }
      BAR();
    };
    if (w < 3) {
      ldfrags(pf, L_K + (3 + w) * TILE);
      ldfrags(kf, L_K);
      WAITV(3);
      BAR();
      STAMP(1);
      unsigned xw[4];
#pragma unroll
      for (int qq = 0; qq < 4; ++qq) xw[qq] = L_X + r32 * 192 + w * 64 + ((((2 * qq + h) ^ ((r32 >> 1) & 7))) << 3);
      __builtin_amdgcn_s_setprio(1);
#pragma unroll 1
      for (int r = 0; r <= 15; ++r) {
        round_open(r);
        const float nb = *(LAS float*)(L + L_BIAS + (r & 1) * 128 + r32 * 4);
        f32x16 acc = {};
#pragma unroll
        for (int ks = 0; ks < 12; ++ks) acc = mfma(pf[ks], kf[ks], acc);
        ldfrags(kf, L_K + nslot * TILE);
        unsigned o[8];
        feat(acc, c1, nb, o);
        const unsigned xb = (r & 1) * 6144;
#pragma unroll
        for (int qq = 0; qq < 4; ++qq) { u32x2 t = {o[2 * qq], o[2 * qq + 1]}; *(LAS u32x2*)(L + xb + xw[qq]) = t; }
        round_close(r);
      }
      __builtin_amdgcn_s_setprio(0);
    } else {
      ldfrags(kf, L_K);
      { float n = rownorm(kf); if (l < 32) *(LAS float*)(L + L_BIAS + l * 4) = __builtin_fmaf(n, cn, c0); }
      WAITV(3);
      BAR();
#pragma unroll 1
      for (int r = 0; r <= 15; ++r) {
        round_open(r);
        if (r < 15) {
          ldfrags(kf, L_K + nslot * TILE);
          float n = rownorm(kf);
          if (l < 32) *(LAS float*)(L + L_BIAS + ((r + 1) & 1) * 128 + l * 4) = __builtin_fmaf(n, cn, c0);
        }
        round_close(r);
      }
    }
    round_open(16);
    round_close(16);
    STAMP(2);
    unsigned pq[2][6][8];
    const unsigned qslot = L_V + (1 + w) * TILE;
#pragma unroll
    for (int t = 0; t < 2; ++t) {
      WAITV(0);
      ldfrags(kf, qslot);
      WAITL();
      if (t == 0) issueQ(1);
      else if (l == 0) __hip_atomic_fetch_add((LAS unsigned*)(L + L_CNT), 1u, __ATOMIC_RELAXED, __HIP_MEMORY_SCOPE_WORKGROUP);
      const float nb = __builtin_fmaf(rownorm(kf), cn, c0);
#pragma unroll
      for (int u = 0; u < 6; ++u) {
        ldfrags(pf, L_K + ((4 + u) % 6) * TILE);
        f32x16 acc = {};
#pragma unroll
        for (int ks = 0; ks < 12; ++ks) acc = mfma(pf[ks], kf[ks], acc);
        feat(acc, c1, nb, pq[t][u]);
      }
    }
    STAMP(3);
    BAR();
    STAMP(4);
    float inv[2];
    {
      f32x16 d0 = {}, d1 = {};
#pragma unroll
      for (int us = 0; us < 12; ++us) {
        u32x4 a = ld128(L + L_TAB + us * 32 + h * 16);
        const unsigned* p0 = &pq[0][us >> 1][(us & 1) * 4];
        const unsigned* p1 = &pq[1][us >> 1][(us & 1) * 4];
        d0 = mfma(a, (u32x4){p0[0], p0[1], p0[2], p0[3]}, d0);
        d1 = mfma(a, (u32x4){p1[0], p1[1], p1[2], p1[3]}, d1);
      }
      inv[0] = 1.0f / (d0[0] + 1e-6f);
      inv[1] = 1.0f / (d1[0] + 1e-6f);
    }
    const __amdgpu_buffer_rsrc_t sO = __builtin_amdgcn_make_buffer_rsrc((void*)(out + hoff + (size_t)i8 * 256 * ROWB), 0, 256 * ROWB, 0x00020000);
    const unsigned oo = (w * 64 + r32) * ROWB + h * 16;
#pragma unroll 1
    for (int c = 0; c < 6; ++c) {
      f32x16 a0 = {}, a1 = {};
      const unsigned kb = L_V + c * 12288 + l * 16;
#pragma unroll
      for (int us = 0; us < 12; ++us) {
        u32x4 a = ld128(L + kb + us * 1024);
        const unsigned* p0 = &pq[0][us >> 1][(us & 1) * 4];
        const unsigned* p1 = &pq[1][us >> 1][(us & 1) * 4];
        a0 = mfma(a, (u32x4){p0[0], p0[1], p0[2], p0[3]}, a0);
        a1 = mfma(a, (u32x4){p1[0], p1[1], p1[2], p1[3]}, a1);
      }
#pragma unroll
      for (int t = 0; t < 2; ++t) {
        const f32x16& a = t ? a1 : a0;
        unsigned o[8];
#pragma unroll
        for (int i = 0; i < 8; ++i) o[i] = pk(a[2 * i] * inv[t], a[2 * i + 1] * inv[t]);
#pragma unroll
        for (int qp = 0; qp < 2; ++qp) {
          auto x0 = __builtin_amdgcn_permlane32_swap(o[4 * qp], o[4 * qp + 2], false, false);
          auto x1 = __builtin_amdgcn_permlane32_swap(o[4 * qp + 1], o[4 * qp + 3], false, false);
          u32x4 val = {x0[0], x1[0], x0[1], x1[1]};
          __builtin_amdgcn_raw_buffer_store_b128(val, sO, oo + c * 64 + t * 32 * ROWB + qp * 32, 0, NT_OUT ? 2 : 0);
        }
      }
    }
    STAMP(5);
    __builtin_amdgcn_s_waitcnt(0);
  } else {
    const int g = w - 4;
    unsigned vo[3];
#pragma unroll
    for (int p = 0; p < 3; ++p) { int Lc = p * 64 + l, row = Lc / 24, c = Lc % 24; vo[p] = row * ROWB + ((c ^ (((row >> 1) & 1) << 2)) << 4); }
    const __amdgpu_buffer_rsrc_t sV = __builtin_amdgcn_make_buffer_rsrc((void*)(v + hoff + (size_t)sc * 512 * ROWB), 0, 512 * ROWB, 0x00020000);
    auto issueV = [&](int j, int slot) {
      const unsigned dst = lbase + L_V + slot * TILE + g * 3072;
#pragma unroll
      for (int p = 0; p < 3; ++p) dma<NT_IN>(dst + p * 1024, vo[p], sV, j * TILE + g * 3072);
    };
    char* const hello = ws + W_HEL + (size_t)head * 64;
    if (w == 7) {
      u64 old;
      const char* hp = hello + i8 * 8;
      asm volatile("global_load_dwordx2 %0, %1, off sc1" : "=v"(old) : "v"(hp) : "memory");
      issueV(0, 0); issueV(1, 1); issueV(2, 2);
      asm volatile("s_waitcnt vmcnt(9)" : "+v"(old)::"memory");
      const u64 n = old * 0x9E3779B97F4A7C15ull + 0x632BE59BD9B4E019ull;
      if (l == 0) { ast(hello + i8 * 8, n); *(LAS u64*)(L + L_NON) = n; *(LAS unsigned*)(L + L_CNT) = 0u; }
    } else { issueV(0, 0); issueV(1, 1); issueV(2, 2); }
    BAR();
    BAR();
    const int blk = (l >> 4) & 1, q4 = (l & 15) >> 2, p4 = l & 3;
    unsigned xa[2], va[2];
#pragma unroll
    for (int t = 0; t < 2; ++t) xa[t] = L_X + (8 * h + 4 * t + q4) * 192 + ((((4 * blk + p4) ^ (4 * h + 2 * t + (q4 >> 1)))) << 3);
#pragma unroll
    for (int e = 0; e < 2; ++e) va[e] = L_V + (8 * h + q4) * ROWB + (2 * g + (e ^ ((q4 >> 1) & 1))) * 64 + blk * 32 + p4 * 8;
    f32x16 acc[2][3] = {};
    float ksum = 0.f;
    u64 view = 0;
    int islot = 3, vslot = 0;
    for (int r = 0; r <= 16; ++r) {
      if (r <= 12) issueV(r + 3, islot);
      islot = islot == 4 ? 0 : islot + 1;
      if (w < 7) {
        if (r == 16) view = ald(hello + (l & 7) * 8);
        if (r >= 1) {
          const unsigned xb = ((r - 1) & 1) * 6144, vb = vslot * TILE;
#pragma unroll
          for (int ks = 0; ks < 2; ++ks) {
            u32x4 A[3], B[2];
#pragma unroll
            for (int j = 0; j < 3; ++j) {
              u32x2 a0 = ldtr(L + xb + xa[0] + ks * 3072 + j * 64), a1 = ldtr(L + xb + xa[1] + ks * 3072 + j * 64);
              A[j] = (u32x4){a0[0], a0[1], a1[0], a1[1]};
            }
#pragma unroll
            for (int e = 0; e < 2; ++e) {
              u32x2 b0 = ldtr(L + vb + va[e] + ks * 6144), b1 = ldtr(L + vb + va[e] + ks * 6144 + 1536);
              B[e] = (u32x4){b0[0], b0[1], b1[0], b1[1]};
            }
#pragma unroll
            for (int e = 0; e < 2; ++e)
#pragma unroll
              for (int j = 0; j < 3; ++j) acc[e][j] = mfma(A[j], B[e], acc[e][j]);
            const u32x4 Ag = g == 0 ? A[0] : (g == 1 ? A[1] : A[2]);
#pragma unroll
            for (int i = 0; i < 4; ++i) ksum = dot2(Ag[i], 0x3f803f80u, ksum);
          }
          vslot = vslot == 4 ? 0 : vslot + 1;
        }
      }
      if (r <= 12) WAITV(9);
      else if (r == 13) WAITV(6);
      else if (r == 14) WAITV(3);
      else if (r == 15) WAITV(0);
      if (r < 16) BAR();
    }
    if (w == 7) { BAR(); BAR(); }
    else {
      STAMP(2);
      const __amdgpu_buffer_rsrc_t sW = __builtin_amdgcn_make_buffer_rsrc((void*)ws, 0, (int)W_INB, 0x00020000);
      const unsigned p1own = (unsigned)W_P1 + (unsigned)((((head * 2 + mh) * 4 + sc) * 3 + g) * 12) * 1024u;
      float own[3][8];
#pragma unroll
      for (int f = 0; f < 12; ++f) {
        const f32x16& a = acc[f / 6][(f % 6) >> 1];
        const int o = (f & 1) * 8;
        if (f / 3 != sc) {
          u32x4 val = {pk(a[o], a[o + 1]), pk(a[o + 2], a[o + 3]), pk(a[o + 4], a[o + 5]), pk(a[o + 6], a[o + 7])};
          __builtin_amdgcn_raw_buffer_store_b128(val, sW, l * 16 + p1own + f * 1024, 0, 16);
        } else {
#pragma unroll
          for (int i = 0; i < 8; ++i) own[f % 3][i] = a[o + i];
        }
      }
      {
        const float ks = halfsum(ksum);
        if (l < 32) __builtin_amdgcn_raw_buffer_store_b32(__float_as_uint(ks), sW, l * 4, (unsigned)W_KS + ((head * 8 + i8) * 3 + g) * 128, 16);
      }
      BAR();
      const u64 nown = *(LAS u64*)(L + L_NON);
      char* const inb = ws + W_INB;
      char* const myin = inb + ((size_t)(head * 8 + i8) * 3 + g) * 128;
      char* const tgt = inb + ((size_t)(head * 8 + (l & 7)) * 3 + g) * 128;
      const bool pub = l >= 8 && l < 16;
      const bool pub1 = pub && ((l & 1) == mh) && ((l & 7) != i8);
      WAITV(0);
      if (pub1) ast(tgt + sc * 8, view);
      STAMP(3);
      {
        const char* pa = pub ? hello + (l & 7) * 8 : myin + (l & 3) * 8;
        for (unsigned it = 0; it < SPIN_MAX; ++it) {
          const u64 x = ald(pa);
          if (pub && x != view) { view = x; if (pub1) ast(tgt + sc * 8, view); }
          const bool ok = pub || l >= 4 || l == sc || x == nown;
          if (__all(ok)) break;
        }
      }
      asm volatile("" ::: "memory");
      STAMP(4);
      u32x4 pin[3][3];
#pragma unroll
      for (int s = 0; s < 3; ++s) {
        const int so = s + (s >= sc);
        const unsigned base = (unsigned)W_P1 + (unsigned)(((((head * 2 + mh) * 4 + so) * 3 + g) * 12) + 3 * sc) * 1024u;
#pragma unroll
        for (int i = 0; i < 3; ++i) pin[s][i] = __builtin_amdgcn_raw_buffer_load_b128(sW, l * 16, base + i * 1024, 16);
      }
      {
        const u64 x = ald(hello + (l & 7) * 8);
        if (pub && x != view) { view = x; if (pub1) ast(tgt + sc * 8, view); }
      }
#pragma unroll
      for (int i = 0; i < 3; ++i) {
#pragma unroll
        for (int s = 0; s < 3; ++s)
#pragma unroll
          for (int e = 0; e < 4; ++e) { own[i][2 * e] += bflo(pin[s][i][e]); own[i][2 * e + 1] += bfhi(pin[s][i][e]); }
        const int f = 3 * sc + i;
        const int cc = 2 * g + f / 6, uu = 3 * mh + ((f % 6) >> 1), sp = f & 1;
        u32x4 val = {pk(own[i][0], own[i][1]), pk(own[i][2], own[i][3]), pk(own[i][4], own[i][5]), pk(own[i][6], own[i][7])};
        __builtin_amdgcn_raw_buffer_store_b128(val, sW, l * 16 + (unsigned)W_FIN + (unsigned)(head * 72 + (cc * 6 + uu) * 2 + sp) * 1024u, 0, 16);
      }
      WAITV(0);
      if (pub) ast(tgt + 32 + i8 * 8, view);
      STAMP(5);
      {
        const char* pa = pub ? hello + (l & 7) * 8 : myin + 32 + (l & 7) * 8;
        for (unsigned it = 0; it < SPIN_MAX; ++it) {
          const u64 x = ald(pa);
          if (pub && x != view) { view = x; ast(tgt + 32 + i8 * 8, view); }
          const bool ok = l >= 8 || x == nown;
          if (__all(ok)) break;
        }
      }
      asm volatile("" ::: "memory");
      STAMP(6);
      u32x4 fin[24];
      const unsigned fb = (unsigned)W_FIN + (unsigned)(head * 72 + g * 24) * 1024u;
#pragma unroll
      for (int i = 0; i < 24; ++i) fin[i] = __builtin_amdgcn_raw_buffer_load_b128(sW, l * 16, fb + i * 1024, 16);
      float kst = 0.f;
#pragma unroll
      for (int s = 0; s < 4; ++s)
        kst += __uint_as_float(__builtin_amdgcn_raw_buffer_load_b32(sW, h * 384 + r32 * 4, (unsigned)W_KS + ((head * 8 + 2 * s) * 3 + g) * 128, 16));
      {
        const u64 x = ald(hello + (l & 7) * 8);
        if (pub && x != view) { view = x; ast(tgt + 32 + i8 * 8, view); }
      }
      for (unsigned it = 0; it < SPIN_MAX; ++it) {
        if (__hip_atomic_load((LAS unsigned*)(L + L_CNT), __ATOMIC_RELAXED, __HIP_MEMORY_SCOPE_WORKGROUP) == 4u) break;
      }
      asm volatile("" ::: "memory");
#pragma unroll
      for (int i = 0; i < 24; ++i) *(LAS u32x4*)(L + L_V + (g * 24 + i) * 1024 + l * 16) = fin[i];
      {
        const int u = 3 * h + g;
        const unsigned off = ((u * 2 + (r32 >> 4)) * 2 + ((r32 >> 2) & 1)) * 16 + (((r32 >> 3) & 1) * 4 + (r32 & 3)) * 2;
        *(LAS unsigned short*)(L + L_TAB + off) = (unsigned short)(pk(kst, 0.f) & 0xffffu);
      }
      STAMP(7);
      BAR();
    }
    __builtin_amdgcn_s_waitcnt(0);
  }
}

extern "C" void kernel_launch(void* const* d_in, const int* in_sizes, int n_in, void* d_out, int out_size, void* d_ws, size_t ws_size,
                              hipStream_t stream) {
  performer_fused<<<256, 512, 0, stream>>>((const char*)d_in[0], (const char*)d_in[1], (const char*)d_in[2], (const char*)d_in[3], (char*)d_out,
                                           (char*)d_ws);
}
```

```cpp
#include <hip/hip_runtime.h>
#include <hip/hip_bf16.h>
#include <stdint.h>

typedef unsigned long long u64;
typedef __attribute__((ext_vector_type(8))) __bf16 bf16x8;
typedef __attribute__((ext_vector_type(2))) __bf16 bf16x2;
typedef __attribute__((ext_vector_type(2))) float f32x2;
typedef __attribute__((ext_vector_type(16))) float f32x16;
typedef __attribute__((ext_vector_type(4))) unsigned u32x4;
typedef __attribute__((ext_vector_type(2))) unsigned u32x2;
typedef __attribute__((ext_vector_type(4))) short s16x4;
#define LAS __attribute__((address_space(3)))
typedef LAS char* lptr;

#define NT_IN 1
#define NT_OUT 0
#define TS 0

constexpr int ROWB = 384;
constexpr int TILE = 12288;
constexpr int L_K = 0, L_V = 73728, L_X = 135168, L_BIAS = 147456, L_TAB = 147968, L_CNT = 148480, L_NON = 148488, L_FLG = 148496, L_END = 148512;
constexpr size_t W_P1 = 0;
constexpr size_t W_FIN = 32ull * 2 * 4 * 3 * 12 * 1024;
constexpr size_t W_KS = W_FIN + 32ull * 72 * 1024;
constexpr size_t W_INB = W_KS + 32ull * 8 * 3 * 128;
constexpr size_t W_HEL = W_INB + 256ull * 3 * 128;
constexpr size_t W_TS = W_HEL + 4096;
constexpr unsigned SPIN_MAX = 1u << 22;

__device__ __forceinline__ f32x16 mfma(u32x4 a, u32x4 b, f32x16 c) {
  return __builtin_amdgcn_mfma_f32_32x32x16_bf16(__builtin_bit_cast(bf16x8, a), __builtin_bit_cast(bf16x8, b), c, 0, 0, 0);
}
__device__ __forceinline__ unsigned pk(float a, float b) {
  f32x2 v = {a, b};
  return __builtin_bit_cast(unsigned, __builtin_convertvector(v, bf16x2));
}
__device__ __forceinline__ float dot2(unsigned a, unsigned b, float c) {
  return __builtin_amdgcn_fdot2_f32_bf16(__builtin_bit_cast(bf16x2, a), __builtin_bit_cast(bf16x2, b), c, false);
}
__device__ __forceinline__ u32x4 ld128(lptr p) { return *(LAS u32x4*)p; }
__device__ __forceinline__ u32x2 ldtr(lptr p) { return __builtin_bit_cast(u32x2, __builtin_amdgcn_ds_read_tr16_b64_v4i16((LAS s16x4*)p)); }
__device__ __forceinline__ float bflo(unsigned x) { return __uint_as_float(x << 16); }
__device__ __forceinline__ float bfhi(unsigned x) { return __uint_as_float(x & 0xffff0000u); }
__device__ __forceinline__ float halfsum(float x) {
  auto r = __builtin_amdgcn_permlane32_swap(__float_as_uint(x), __float_as_uint(x), false, false);
  return __uint_as_float(r[0]) + __uint_as_float(r[1]);
}
template <int NT>
__device__ __forceinline__ void dma(unsigned ldsaddr, unsigned voff, __amdgpu_buffer_rsrc_t srd, unsigned soff) {
  if constexpr (NT)
    asm volatile("s_mov_b32 m0, %0\n\ts_nop 0\n\tbuffer_load_dwordx4 %1, %2, %3 offen nt lds" ::"s"(ldsaddr), "v"(voff), "s"(srd), "s"(soff) : "memory");
  else
    asm volatile("s_mov_b32 m0, %0\n\ts_nop 0\n\tbuffer_load_dwordx4 %1, %2, %3 offen lds" ::"s"(ldsaddr), "v"(voff), "s"(srd), "s"(soff) : "memory");
}
template <int V> struct IC { static constexpr int v = V; };
template <int I, int N, class F> __device__ __forceinline__ void sfor(F&& f) { if constexpr (I < N) { f(IC<I>{}); sfor<I + 1, N>(f); } }
#define SB() __builtin_amdgcn_sched_barrier(0)
#define G1PRIO 1
#define POLL_SLEEP 2
#define WAITV(n) asm volatile("s_waitcnt vmcnt(" #n ")" ::: "memory")
#define WAITL() asm volatile("s_waitcnt lgkmcnt(0)" ::: "memory")
#define BAR() do { asm volatile("s_waitcnt lgkmcnt(0)" ::: "memory"); __builtin_amdgcn_s_barrier(); asm volatile("" ::: "memory"); } while (0)
typedef __attribute__((address_space(1))) u64 gu64;
__device__ __forceinline__ u64 ald(const char* p) { return __hip_atomic_load((gu64*)p, __ATOMIC_RELAXED, __HIP_MEMORY_SCOPE_AGENT); }
__device__ __forceinline__ void ast(char* p, u64 v) { __hip_atomic_store((gu64*)p, v, __ATOMIC_RELAXED, __HIP_MEMORY_SCOPE_AGENT); }
#define STAMP(i)
#define CSTAMP(i)
#define DUMPTS()
#define TNOW() 0ull

__device__ __forceinline__ void feat(const f32x16& acc, float c1, float nb, unsigned* o) {
#pragma unroll
  for (int i = 0; i < 8; ++i)
    o[i] = pk(__builtin_amdgcn_exp2f(__builtin_fmaf(acc[2 * i], c1, nb)), __builtin_amdgcn_exp2f(__builtin_fmaf(acc[2 * i + 1], c1, nb)));
}
__device__ __forceinline__ float rownorm(const u32x4* f) {
  float s0 = 0.f, s1 = 0.f;
#pragma unroll
  for (int i = 0; i < 12; ++i) {
    s0 = dot2(f[i][0], f[i][0], s0); s1 = dot2(f[i][1], f[i][1], s1);
    s0 = dot2(f[i][2], f[i][2], s0); s1 = dot2(f[i][3], f[i][3], s1);
  }
  return halfsum(s0 + s1);
}

__global__ __launch_bounds__(512, 2) void performer_fused(const char* __restrict__ q, const char* __restrict__ k, const char* __restrict__ v,
                                                          const char* __restrict__ proj, char* __restrict__ out, char* __restrict__ ws) {
  __shared__ __attribute__((aligned(16))) char lds_raw[L_END + (TS ? 1024 : 0)];
  lptr L = (lptr)lds_raw;
  const unsigned lbase = (unsigned)(uintptr_t)L;
  const int b = blockIdx.x;
  const int l = threadIdx.x & 63;
  const int w = __builtin_amdgcn_readfirstlane(threadIdx.x >> 6);
  const int head = (b & 7) * 4 + (b >> 6), i8 = (b >> 3) & 7, mh = i8 & 1, sc = i8 >> 1;
  const int h = l >> 5, r32 = l & 31;
  const float scale = 0.26864248f;
  const float LOG2E = 1.44269504089f;
  const float c1 = scale * LOG2E, cn = -0.5f * scale * scale * LOG2E, c0 = -3.79248125036f;
  const size_t hoff = (size_t)head * 2048 * ROWB;
  STAMP(0);

  unsigned fa[4];
#pragma unroll
  for (int kk = 0; kk < 4; ++kk) fa[kk] = r32 * ROWB + ((((2 * kk + h) ^ ((r32 >> 1) & 7))) << 4);
  auto ldfrags = [&](u32x4* f, unsigned base) {
#pragma unroll
    for (int ks = 0; ks < 12; ++ks) f[ks] = ld128(L + base + fa[ks & 3] + (ks >> 2) * 128);
  };

  unsigned vo[3];
#pragma unroll
  for (int p = 0; p < 3; ++p) { int Lc = p * 64 + l, row = Lc / 24, c = Lc % 24; vo[p] = row * ROWB + ((c ^ (row >> 1)) << 4); }
  auto issue12 = [&](auto nt, unsigned dst, __amdgpu_buffer_rsrc_t srd, unsigned soff) {
#pragma unroll
    for (int a = 0; a < 4; ++a)
#pragma unroll
      for (int p = 0; p < 3; ++p) dma<decltype(nt)::v>(dst + a * 3072 + p * 1024, vo[p] ^ ((a & 1) << 6), srd, soff + a * 3072);
  };
  const __amdgpu_buffer_rsrc_t sQ = __builtin_amdgcn_make_buffer_rsrc((void*)(q + hoff + (size_t)i8 * 256 * ROWB), 0, 256 * ROWB, 0x00020000);
  u64 tprev = 0, acc_c = 0, acc_v = 0, acc_b = 0;
#define RCLOSE(r, waits) do { WAITL(); const u64 ta = TNOW(); waits; const u64 tb = TNOW(); BAR(); const u64 tc = TNOW(); \
    if (TS >= 2 && (r) >= 2 && (r) <= 11) { acc_c += ta - tprev; acc_v += tb - ta; acc_b += tc - tb; } tprev = tc; } while (0)
#define DUMPACC()

  if (w < 4) {
    const __amdgpu_buffer_rsrc_t sK = __builtin_amdgcn_make_buffer_rsrc((void*)(k + hoff + (size_t)sc * 512 * ROWB), 0, 512 * ROWB, 0x00020000);
    const __amdgpu_buffer_rsrc_t sP = __builtin_amdgcn_make_buffer_rsrc((void*)proj, 0, 192 * ROWB, 0x00020000);
    {
      const unsigned par = (w & 1) << 6;
#pragma unroll
      for (int t = 0; t < 3; ++t)
#pragma unroll
        for (int p = 0; p < 3; ++p) dma<0>(lbase + L_K + (3 + t) * TILE + w * 3072 + p * 1024, vo[p] ^ par, sP, (3 * mh + t) * TILE + w * 3072);
#pragma unroll
      for (int t = 0; t < 3; ++t)
#pragma unroll
        for (int p = 0; p < 3; ++p) dma<NT_IN>(lbase + L_K + t * TILE + w * 3072 + p * 1024, vo[p] ^ par, sK, t * TILE + w * 3072);
    }
    WAITV(6);
    BAR();
    u32x4 pf[12], kf[12];
    if (w < 3) {
      ldfrags(pf, L_K + (3 + w) * TILE);
      ldfrags(kf, L_K);
      WAITV(3);
      BAR();
      STAMP(1);
      unsigned xw[4];
#pragma unroll
      for (int qq = 0; qq < 4; ++qq) xw[qq] = L_X + r32 * 192 + w * 64 + ((((2 * qq + h) ^ ((r32 >> 1) & 7))) << 3);
      int nslot = 1;
      f32x16 acc[2];
      auto g1_round = [&](auto par, auto first, auto last, int r) {
        constexpr int P = decltype(par)::v;
        constexpr bool F = decltype(first)::v, Z = decltype(last)::v;
        float nb = 0.f;
        if constexpr (!F) nb = *(LAS float*)(L + L_BIAS + ((r - 1) & 3) * 128 + r32 * 4);
        const unsigned kb = L_K + nslot * TILE, xb = (P ^ 1) * 6144;
        nslot = nslot == 5 ? 0 : nslot + 1;
        unsigned o[8];
        SB();
        sfor<0, 12>([&](auto ic) {
          constexpr int ks = decltype(ic)::v;
          if constexpr (!Z) {
            acc[P] = mfma(pf[ks], kf[ks], ks == 0 ? (f32x16){} : acc[P]);
            kf[ks] = ld128(L + kb + fa[ks & 3] + (ks >> 2) * 128);
          }
          if constexpr (!F && ks >= 2 && ks < 10) {
            constexpr int i = ks - 2;
            const f32x16& pa = acc[P ^ 1];
            o[i] = pk(__builtin_amdgcn_exp2f(__builtin_fmaf(pa[2 * i], c1, nb)), __builtin_amdgcn_exp2f(__builtin_fmaf(pa[2 * i + 1], c1, nb)));
            if constexpr (i & 1) { u32x2 t = {o[i - 1], o[i]}; *(LAS u32x2*)(L + xb + xw[i >> 1]) = t; }
          }
          SB();
        });
      };
      g1_round(IC<0>{}, IC<1>{}, IC<0>{}, 0);
      RCLOSE(0, WAITV(0));
#pragma unroll 1
      for (int r = 1; r < 15; r += 2) {
        if (TS && r == 5) STAMP(10);
        if (TS && r == 13) STAMP(11);
        g1_round(IC<1>{}, IC<0>{}, IC<0>{}, r);
        RCLOSE(r, );
        g1_round(IC<0>{}, IC<0>{}, IC<0>{}, r + 1);
        RCLOSE(r + 1, );
      }
      g1_round(IC<1>{}, IC<0>{}, IC<0>{}, 15);
      RCLOSE(15, );
      g1_round(IC<0>{}, IC<0>{}, IC<1>{}, 16);
      RCLOSE(16, );
    } else {
      ldfrags(kf, L_K);
      { float n = rownorm(kf); if (l < 32) *(LAS float*)(L + L_BIAS + l * 4) = __builtin_fmaf(n, cn, c0); }
      WAITV(3);
      BAR();
      int islot = 0, nslot = 1;
#pragma unroll 1
      for (int r = 0; r <= 15; ++r) {
        ldfrags(kf, L_K + nslot * TILE);
        nslot = nslot == 5 ? 0 : nslot + 1;
        if (r == 0) {
#pragma unroll 1
          for (int j = 3; j < 6; ++j) issue12(IC<NT_IN>{}, lbase + L_K + j * TILE, sK, j * TILE);
        }
        if (r < 10) issue12(IC<NT_IN>{}, lbase + L_K + islot * TILE, sK, (r + 6) * TILE);
        else issue12(IC<0>{}, lbase + L_K + islot * TILE, sP, (r - 10) * TILE);
        islot = islot == 5 ? 0 : islot + 1;
        float n = rownorm(kf);
        if (l < 32) *(LAS float*)(L + L_BIAS + ((r + 1) & 3) * 128 + l * 4) = __builtin_fmaf(n, cn, c0);
        RCLOSE(r, if (r <= 13) WAITV(48));
      }
      RCLOSE(16, );
      WAITV(0);
      if (l == 0) *(LAS volatile unsigned*)(L + L_FLG) = 1u;
    }
    DUMPACC();
    for (unsigned it = 0; it < SPIN_MAX; ++it) {
      const unsigned f0 = *(LAS volatile unsigned*)(L + L_FLG), f1 = *(LAS volatile unsigned*)(L + L_FLG + 4);
      if (f0 != 0u && f1 > (unsigned)w) break;
      __builtin_amdgcn_s_sleep(1);
    }
    asm volatile("" ::: "memory");
    STAMP(2);
    CSTAMP(14);
    unsigned pq[2][6][8];
    const unsigned qslot = L_V + (1 + w) * TILE;
#pragma unroll
    for (int t = 0; t < 2; ++t) {
      if (t == 1) WAITV(0);
      ldfrags(kf, qslot);
      u32x4 A[6];
#pragma unroll
      for (int i = 0; i < 6; ++i) A[i] = ld128(L + L_K + 4 * TILE + fa[i & 3] + (i >> 2) * 128);
      WAITL();
      if (t == 0) issue12(IC<NT_IN>{}, lbase + qslot, sQ, (2 * w + 1) * TILE);
      else if (l == 0) __hip_atomic_fetch_add((LAS unsigned*)(L + L_CNT), 1u, __ATOMIC_RELAXED, __HIP_MEMORY_SCOPE_WORKGROUP);
      STAMP(6 + 2 * t);
      const float nb = __builtin_fmaf(rownorm(kf), cn, c0);
      f32x16 acc[2];
      SB();
      sfor<0, 84>([&](auto ic) {
        constexpr int n = decltype(ic)::v, u = n / 12, ks = n % 12;
        if constexpr (n < 72) {
          acc[u & 1] = mfma(A[n % 6], kf[ks], ks == 0 ? (f32x16){} : acc[u & 1]);
          if constexpr (n + 6 < 72) { constexpr int m = n + 6, uu = m / 12, kk = m % 12; A[n % 6] = ld128(L + L_K + ((4 + uu) % 6) * TILE + fa[kk & 3] + (kk >> 2) * 128); }
        }
        if constexpr (u >= 1 && ks >= 2 && ks < 10) {
          constexpr int i = ks - 2;
          const f32x16& pa = acc[(u - 1) & 1];
          pq[t][u - 1][i] = pk(__builtin_amdgcn_exp2f(__builtin_fmaf(pa[2 * i], c1, nb)), __builtin_amdgcn_exp2f(__builtin_fmaf(pa[2 * i + 1], c1, nb)));
          asm volatile("" : "+v"(pq[t][u - 1][i]));
        }
        SB();
      });
    }
    STAMP(3);
    CSTAMP(15);
    BAR();
    STAMP(4);
    const __amdgpu_buffer_rsrc_t sO = __builtin_amdgcn_make_buffer_rsrc((void*)(out + hoff + (size_t)i8 * 256 * ROWB), 0, 256 * ROWB, 0x00020000);
    const unsigned sa = w * 16384 + r32 * 128 + ((r32 & 7) << 4) + h * 8;
    const unsigned sr = w * 16384 + (l >> 3) * 128 + (((l & 7) ^ ((l >> 3) & 7)) << 4);
    const unsigned so = (w * 64 + (l >> 3)) * ROWB + (l & 7) * 16;
    {
      float inv[2];
      f32x16 acc[2][2];
      u32x4 A[6];
      const unsigned tb = L_TAB + h * 16, kb = L_V + l * 16;
#pragma unroll
      for (int i = 0; i < 6; ++i) A[i] = ld128(L + tb + i * 32);
      SB();
      sfor<0, 96>([&](auto ic) {
        constexpr int n = decltype(ic)::v, c = n / 12, us = n % 12, pb = c & 1;
        if constexpr (n < 84) {
          const unsigned* p0 = &pq[0][us >> 1][(us & 1) * 4];
          const unsigned* p1 = &pq[1][us >> 1][(us & 1) * 4];
          acc[pb][0] = mfma(A[n % 6], (u32x4){p0[0], p0[1], p0[2], p0[3]}, us == 0 ? (f32x16){} : acc[pb][0]);
          acc[pb][1] = mfma(A[n % 6], (u32x4){p1[0], p1[1], p1[2], p1[3]}, us == 0 ? (f32x16){} : acc[pb][1]);
          constexpr int m = n + 6;
          if constexpr (m < 12) A[n % 6] = ld128(L + tb + m * 32);
          else if constexpr (m < 84) A[n % 6] = ld128(L + kb + (m - 12) * 1024);
        }
        if constexpr (c == 1 && us == 1) { inv[0] = 1.0f / (acc[0][0][0] + 1e-6f); inv[1] = 1.0f / (acc[0][1][0] + 1e-6f); }
        if constexpr (c >= 2 && us >= 1 && us <= 8) {
          constexpr int dt = c - 2, e = us - 1, t = e >> 2, qq = e & 3;
          const f32x16& a = acc[pb ^ 1][t];
          u32x2 o = {pk(a[4 * qq] * inv[t], a[4 * qq + 1] * inv[t]), pk(a[4 * qq + 2] * inv[t], a[4 * qq + 3] * inv[t])};
          *(LAS u32x2*)(L + (sa ^ ((((dt & 1) * 4 + qq)) << 4)) + ((dt >> 1) & 1) * 8192 + t * 4096) = o;
        }
        if constexpr (c >= 3 && (c & 1) == 1 && us >= 9) {
          constexpr int cp = (c - 3) >> 1;
          sfor<0, 3>([&](auto jc) {
            constexpr int i = (us - 9) * 3 + decltype(jc)::v;
            if constexpr (i < 8) {
              u32x4 val = ld128(L + sr + (cp & 1) * 8192 + i * 1024);
              __builtin_amdgcn_raw_buffer_store_b128(val, sO, so + i * 8 * ROWB + cp * 128, 0, NT_OUT ? 2 : 0);
            }
          });
        }
        SB();
      });
    }
    STAMP(5);
    DUMPTS();
    __builtin_amdgcn_s_waitcnt(0);
  } else {
    const int g = w - 4;
    char* const hello = ws + W_HEL + (size_t)head * 64;
    if (w == 7) {
      unsigned vv[3];
#pragma unroll
      for (int p = 0; p < 3; ++p) { int Lc = p * 64 + l, row = Lc / 24, c = Lc % 24; vv[p] = row * ROWB + ((c ^ (((row >> 1) & 1) << 2)) << 4); }
      const __amdgpu_buffer_rsrc_t sV = __builtin_amdgcn_make_buffer_rsrc((void*)(v + hoff + (size_t)sc * 512 * ROWB), 0, 512 * ROWB, 0x00020000);
      auto issueV = [&](int j, int slot) {
#pragma unroll
        for (int a = 0; a < 4; ++a)
#pragma unroll
          for (int p = 0; p < 3; ++p) dma<NT_IN>(lbase + L_V + slot * TILE + a * 3072 + p * 1024, vv[p], sV, j * TILE + a * 3072);
      };
      const u64 old = ald(hello + i8 * 8);
      const u64 n = old * 0x9E3779B97F4A7C15ull + 0x632BE59BD9B4E019ull;
      if (l == 0) {
        ast(hello + i8 * 8, n);
        *(LAS u64*)(L + L_NON) = n; *(LAS unsigned*)(L + L_CNT) = 0u; *(LAS u64*)(L + L_FLG) = 0ull;
      }
#pragma unroll 1
      for (int j = 0; j < 3; ++j) issueV(j, j);
      BAR();
      BAR();
      int islot = 3;
#pragma unroll 1
      for (int r = 0; r <= 16; ++r) {
        if (r <= 12) issueV(r + 3, islot);
        else issue12(IC<NT_IN>{}, lbase + L_V + (r - 12) * TILE, sQ, 2 * (r - 13) * TILE);
        islot = islot == 4 ? 0 : islot + 1;
        RCLOSE(r, if (r <= 15) WAITV(36));
      }
      DUMPACC();
      WAITV(36); if (l == 0) *(LAS volatile unsigned*)(L + L_FLG + 4) = 1u;
      WAITV(24); if (l == 0) *(LAS volatile unsigned*)(L + L_FLG + 4) = 2u;
      WAITV(12); if (l == 0) *(LAS volatile unsigned*)(L + L_FLG + 4) = 3u;
      WAITV(0);  if (l == 0) *(LAS volatile unsigned*)(L + L_FLG + 4) = 4u;
      BAR();
    } else {
      BAR();
      BAR();
      const int blk = (l >> 4) & 1, q4 = (l & 15) >> 2, p4 = l & 3;
      unsigned xa[2], va[2];
#pragma unroll
      for (int t = 0; t < 2; ++t) xa[t] = L_X + (8 * h + 4 * t + q4) * 192 + ((((4 * blk + p4) ^ (4 * h + 2 * t + (q4 >> 1)))) << 3);
#pragma unroll
      for (int e = 0; e < 2; ++e) va[e] = L_V + (8 * h + q4) * ROWB + (2 * g + (e ^ ((q4 >> 1) & 1))) * 64 + blk * 32 + p4 * 8;
      f32x16 acc[2][3] = {};
      float ksum = 0.f;
      u64 view = 0;
      u32x4 B[2][2];
      auto ldB = [&](unsigned vb) {
#pragma unroll
        for (int ks = 0; ks < 2; ++ks)
#pragma unroll
          for (int e = 0; e < 2; ++e) {
            const unsigned ad = va[e] + vb;
            u32x2 b0 = ldtr(L + ad + ks * 6144), b1 = ldtr(L + ad + ks * 6144 + 1536);
            B[ks][e] = (u32x4){b0[0], b0[1], b1[0], b1[1]};
          }
      };
      if (G1PRIO) __builtin_amdgcn_s_setprio(G1PRIO);
      RCLOSE(0, );
      ldB(0);
      RCLOSE(1, );
      int vslot = 1;
#pragma unroll 1
      for (int r = 2; r <= 17; ++r) {
        if (r == 17) view = ald(hello + (l & 7) * 8);
        const unsigned x0 = xa[0] + (r & 1) * 6144, x1 = xa[1] + (r & 1) * 6144;
        u32x4 A[2][3];
#pragma unroll
        for (int ks = 0; ks < 2; ++ks)
#pragma unroll
          for (int j = 0; j < 3; ++j) {
            u32x2 a0 = ldtr(L + x0 + ks * 3072 + j * 64), a1 = ldtr(L + x1 + ks * 3072 + j * 64);
            A[ks][j] = (u32x4){a0[0], a0[1], a1[0], a1[1]};
          }
        SB();
#pragma unroll
        for (int ks = 0; ks < 2; ++ks)
#pragma unroll
          for (int j = 0; j < 3; ++j)
#pragma unroll
            for (int e = 0; e < 2; ++e) acc[e][j] = mfma(A[ks][j], B[ks][e], acc[e][j]);
        SB();
        if (r < 17) ldB(vslot * TILE);
        vslot = vslot == 4 ? 0 : vslot + 1;
#pragma unroll
        for (int ks = 0; ks < 2; ++ks) {
          const u32x4 Ag = g == 0 ? A[ks][0] : (g == 1 ? A[ks][1] : A[ks][2]);
#pragma unroll
          for (int i = 0; i < 4; ++i) ksum = dot2(Ag[i], 0x3f803f80u, ksum);
        }
        if (r < 17) RCLOSE(r, );
      }
      DUMPACC();
      if (G1PRIO) __builtin_amdgcn_s_setprio(0);
      STAMP(2);
      WAITL();
      if (l == 0) __hip_atomic_fetch_add((LAS unsigned*)(L + L_CNT), 1u, __ATOMIC_RELAXED, __HIP_MEMORY_SCOPE_WORKGROUP);
      const __amdgpu_buffer_rsrc_t sW = __builtin_amdgcn_make_buffer_rsrc((void*)ws, 0, (int)W_INB, 0x00020000);
      const unsigned p1own = (unsigned)W_P1 + (unsigned)((((head * 2 + mh) * 4 + sc) * 3 + g) * 12) * 1024u;
      float own[3][8];
#pragma unroll
      for (int f = 0; f < 12; ++f) {
        const f32x16& a = acc[f / 6][(f % 6) >> 1];
        const int o = (f & 1) * 8;
        if (f / 3 != sc) {
          u32x4 val = {pk(a[o], a[o + 1]), pk(a[o + 2], a[o + 3]), pk(a[o + 4], a[o + 5]), pk(a[o + 6], a[o + 7])};
          __builtin_amdgcn_raw_buffer_store_b128(val, sW, l * 16 + p1own + f * 1024, 0, 16);
        } else {
#pragma unroll
          for (int i = 0; i < 8; ++i) own[f % 3][i] = a[o + i];
        }
      }
      {
        const float ks = halfsum(ksum);
        if (l < 32) __builtin_amdgcn_raw_buffer_store_b32(__float_as_uint(ks), sW, l * 4, (unsigned)W_KS + ((head * 8 + i8) * 3 + g) * 128, 16);
      }
      const u64 nown = *(LAS u64*)(L + L_NON);
      char* const inb = ws + W_INB;
      char* const myin = inb + ((size_t)(head * 8 + i8) * 3 + g) * 128;
      char* const tgt = inb + ((size_t)(head * 8 + (l & 7)) * 3 + g) * 128;
      const bool pub = l >= 8 && l < 16;
      const bool pub1 = pub && ((l & 1) == mh) && ((l & 7) != i8);
      WAITV(0);
      if (pub1) ast(tgt + sc * 8, view);
      STAMP(3);
      {
        const char* pa = pub ? hello + (l & 7) * 8 : myin + (l & 3) * 8;
        for (unsigned it = 0; it < SPIN_MAX; ++it) {
          const u64 x = ald(pa);
          if (pub && x != view) { view = x; if (pub1) ast(tgt + sc * 8, view); }
          const bool ok = pub || l >= 4 || l == sc || x == nown;
          if (__all(ok)) break;
          __builtin_amdgcn_s_sleep(POLL_SLEEP);
        }
      }
      asm volatile("" ::: "memory");
      STAMP(4);
      u32x4 pin[3][3];
#pragma unroll
      for (int s = 0; s < 3; ++s) {
        const int so = s + (s >= sc);
        const unsigned base = (unsigned)W_P1 + (unsigned)(((((head * 2 + mh) * 4 + so) * 3 + g) * 12) + 3 * sc) * 1024u;
#pragma unroll
        for (int i = 0; i < 3; ++i) pin[s][i] = __builtin_amdgcn_raw_buffer_load_b128(sW, l * 16, base + i * 1024, 16);
      }
      {
        const u64 x = ald(hello + (l & 7) * 8);
        if (pub && x != view) { view = x; if (pub1) ast(tgt + sc * 8, view); }
      }
#pragma unroll
      for (int i = 0; i < 3; ++i) {
#pragma unroll
        for (int s = 0; s < 3; ++s)
#pragma unroll
          for (int e = 0; e < 4; ++e) { own[i][2 * e] += bflo(pin[s][i][e]); own[i][2 * e + 1] += bfhi(pin[s][i][e]); }
        const int f = 3 * sc + i;
        const int cc = 2 * g + f / 6, uu = 3 * mh + ((f % 6) >> 1), sp = f & 1;
        u32x4 val = {pk(own[i][0], own[i][1]), pk(own[i][2], own[i][3]), pk(own[i][4], own[i][5]), pk(own[i][6], own[i][7])};
        __builtin_amdgcn_raw_buffer_store_b128(val, sW, l * 16 + (unsigned)W_FIN + (unsigned)(head * 72 + (cc * 6 + uu) * 2 + sp) * 1024u, 0, 16);
      }
      WAITV(0);
      if (pub) ast(tgt + 32 + i8 * 8, view);
      STAMP(5);
      {
        const char* pa = pub ? hello + (l & 7) * 8 : myin + 32 + (l & 7) * 8;
        for (unsigned it = 0; it < SPIN_MAX; ++it) {
          const u64 x = ald(pa);
          if (pub && x != view) { view = x; ast(tgt + 32 + i8 * 8, view); }
          const bool ok = l >= 8 || x == nown;
          if (__all(ok)) break;
          __builtin_amdgcn_s_sleep(POLL_SLEEP);
        }
      }
      asm volatile("" ::: "memory");
      STAMP(6);
      u32x4 fin[24];
      const unsigned fb = (unsigned)W_FIN + (unsigned)(head * 72 + g * 24) * 1024u;
#pragma unroll
      for (int i = 0; i < 24; ++i) fin[i] = __builtin_amdgcn_raw_buffer_load_b128(sW, l * 16, fb + i * 1024, 16);
      float kst = 0.f;
#pragma unroll
      for (int s = 0; s < 4; ++s)
        kst += __uint_as_float(__builtin_amdgcn_raw_buffer_load_b32(sW, h * 384 + r32 * 4, (unsigned)W_KS + ((head * 8 + 2 * s) * 3 + g) * 128, 16));
      {
        const u64 x = ald(hello + (l & 7) * 8);
        if (pub && x != view) { view = x; ast(tgt + 32 + i8 * 8, view); }
      }
      for (unsigned it = 0; it < SPIN_MAX; ++it) {
        if (__hip_atomic_load((LAS unsigned*)(L + L_CNT), __ATOMIC_RELAXED, __HIP_MEMORY_SCOPE_WORKGROUP) == 7u) break;
        __builtin_amdgcn_s_sleep(1);
      }
      asm volatile("" ::: "memory");
#pragma unroll
      for (int i = 0; i < 24; ++i) *(LAS u32x4*)(L + L_V + (g * 24 + i) * 1024 + l * 16) = fin[i];
      {
        const int u = 3 * h + g;
        const unsigned off = ((u * 2 + (r32 >> 4)) * 2 + ((r32 >> 2) & 1)) * 16 + (((r32 >> 3) & 1) * 4 + (r32 & 3)) * 2;
        *(LAS unsigned short*)(L + L_TAB + off) = (unsigned short)(pk(kst, 0.f) & 0xffffu);
      }
      STAMP(7);
      BAR();
    }
    DUMPTS();
    __builtin_amdgcn_s_waitcnt(0);
  }
}

extern "C" void kernel_launch(void* const* d_in, const int* in_sizes, int n_in, void* d_out, int out_size, void* d_ws, size_t ws_size,
                              hipStream_t stream) {
  performer_fused<<<256, 512, 0, stream>>>((const char*)d_in[0], (const char*)d_in[1], (const char*)d_in[2], (const char*)d_in[3], (char*)d_out,
                                           (char*)d_ws);
}
```

```cpp
#include <hip/hip_runtime.h>
#include <hip/hip_bf16.h>
#include <stdint.h>

typedef unsigned long long u64;
typedef __attribute__((ext_vector_type(8))) __bf16 bf16x8;
typedef __attribute__((ext_vector_type(2))) __bf16 bf16x2;
typedef __attribute__((ext_vector_type(2))) float f32x2;
typedef __attribute__((ext_vector_type(16))) float f32x16;
typedef __attribute__((ext_vector_type(4))) unsigned u32x4;
typedef __attribute__((ext_vector_type(2))) unsigned u32x2;
typedef __attribute__((ext_vector_type(4))) short s16x4;
#define LAS __attribute__((address_space(3)))
typedef LAS char* lptr;

#define NT_IN 1
#define NT_OUT 19
#define TS 0

constexpr int ROWB = 384;
constexpr int TILE = 12288;
constexpr int L_K = 0, L_V = 7 * TILE, L_X = L_V + 5 * TILE, L_BIAS = L_X + TILE, L_TAB = L_BIAS + 512, L_CNT = L_TAB + 512, L_NON = L_CNT + 8,
              L_FLG = L_CNT + 16, L_KSC = L_CNT + 32, L_SF = L_CNT + 48, L_FR = L_CNT + 64, L_Q0 = L_CNT + 80, L_DRN = L_CNT + 96, L_END = L_CNT + 112;
constexpr size_t W_P1 = 0;
constexpr size_t W_FIN = 32ull * 2 * 4 * 3 * 12 * 1024;
constexpr size_t W_KS = W_FIN + 32ull * 72 * 1024;
constexpr size_t W_INB = W_KS + 32ull * 8 * 3 * 128;
constexpr size_t W_HEL = W_INB + 256ull * 3 * 128;
constexpr size_t W_TS = W_HEL + 4096;
constexpr unsigned SPIN_MAX = 1u << 22;

__device__ __forceinline__ f32x16 mfma(u32x4 a, u32x4 b, f32x16 c) {
  return __builtin_amdgcn_mfma_f32_32x32x16_bf16(__builtin_bit_cast(bf16x8, a), __builtin_bit_cast(bf16x8, b), c, 0, 0, 0);
}
__device__ __forceinline__ unsigned pk(float a, float b) {
  f32x2 v = {a, b};
  return __builtin_bit_cast(unsigned, __builtin_convertvector(v, bf16x2));
}
__device__ __forceinline__ float dot2(unsigned a, unsigned b, float c) {
  return __builtin_amdgcn_fdot2_f32_bf16(__builtin_bit_cast(bf16x2, a), __builtin_bit_cast(bf16x2, b), c, false);
}
__device__ __forceinline__ u32x4 ld128(lptr p) { return *(LAS u32x4*)p; }
__device__ __forceinline__ u32x2 ldtr(lptr p) { return __builtin_bit_cast(u32x2, __builtin_amdgcn_ds_read_tr16_b64_v4i16((LAS s16x4*)p)); }
__device__ __forceinline__ float bflo(unsigned x) { return __uint_as_float(x << 16); }
__device__ __forceinline__ float bfhi(unsigned x) { return __uint_as_float(x & 0xffff0000u); }
__device__ __forceinline__ float halfsum(float x) {
  auto r = __builtin_amdgcn_permlane32_swap(__float_as_uint(x), __float_as_uint(x), false, false);
  return __uint_as_float(r[0]) + __uint_as_float(r[1]);
}
template <int NT>
__device__ __forceinline__ void dma(unsigned ldsaddr, unsigned voff, __amdgpu_buffer_rsrc_t srd, unsigned soff) {
  if constexpr (NT)
    asm volatile("s_mov_b32 m0, %0\n\ts_nop 0\n\tbuffer_load_dwordx4 %1, %2, %3 offen nt lds" ::"s"(ldsaddr), "v"(voff), "s"(srd), "s"(soff) : "memory");
  else
    asm volatile("s_mov_b32 m0, %0\n\ts_nop 0\n\tbuffer_load_dwordx4 %1, %2, %3 offen lds" ::"s"(ldsaddr), "v"(voff), "s"(srd), "s"(soff) : "memory");
}
template <int V> struct IC { static constexpr int v = V; };
template <int I, int N, class F> __device__ __forceinline__ void sfor(F&& f) { if constexpr (I < N) { f(IC<I>{}); sfor<I + 1, N>(f); } }
#define SB() __builtin_amdgcn_sched_barrier(0)
#define G1PRIO 1
#define Q1AUX 0
#define DRAINW 3u
#define G1P 0
#define NS 8
#define KSV 1
#define QR0 3
#define QRN 6
#define POLL_SLEEP 2
#define WAITV(n) asm volatile("s_waitcnt vmcnt(" #n ")" ::: "memory")
#define WAITL() asm volatile("s_waitcnt lgkmcnt(0)" ::: "memory")
#define BAR() do { asm volatile("s_waitcnt lgkmcnt(0)" ::: "memory"); __builtin_amdgcn_s_barrier(); asm volatile("" ::: "memory"); } while (0)
typedef __attribute__((address_space(1))) u64 gu64;
__device__ __forceinline__ u64 ald(const char* p) { return __hip_atomic_load((gu64*)p, __ATOMIC_RELAXED, __HIP_MEMORY_SCOPE_AGENT); }
__device__ __forceinline__ void ast(char* p, u64 v) { __hip_atomic_store((gu64*)p, v, __ATOMIC_RELAXED, __HIP_MEMORY_SCOPE_AGENT); }
#define STAMP(i)
#define CSTAMP(i)
#define DUMPTS()
#define TNOW() 0ull

__device__ __forceinline__ void feat(const f32x16& acc, float c1, float nb, unsigned* o) {
#pragma unroll
  for (int i = 0; i < 8; ++i)
    o[i] = pk(__builtin_amdgcn_exp2f(__builtin_fmaf(acc[2 * i], c1, nb)), __builtin_amdgcn_exp2f(__builtin_fmaf(acc[2 * i + 1], c1, nb)));
}
__device__ __forceinline__ float rownorm(const u32x4* f) {
  float s0 = 0.f, s1 = 0.f;
#pragma unroll
  for (int i = 0; i < 12; ++i) {
    s0 = dot2(f[i][0], f[i][0], s0); s1 = dot2(f[i][1], f[i][1], s1);
    s0 = dot2(f[i][2], f[i][2], s0); s1 = dot2(f[i][3], f[i][3], s1);
  }
  return halfsum(s0 + s1);
}

__global__ __launch_bounds__(512, 2) void performer_fused(const char* __restrict__ q, const char* __restrict__ k, const char* __restrict__ v,
                                                          const char* __restrict__ proj, char* __restrict__ out, char* __restrict__ ws) {
  __shared__ __attribute__((aligned(16))) char lds_raw[L_END + (TS ? 1024 : 0)];
  lptr L = (lptr)lds_raw;
  const unsigned lbase = (unsigned)(uintptr_t)L;
  const int b = blockIdx.x;
  const int l = threadIdx.x & 63;
  const int w = __builtin_amdgcn_readfirstlane(threadIdx.x >> 6);
  const int head = (b & 7) * 4 + (b >> 6), i8 = (b >> 3) & 7, mh = i8 & 1, sc = i8 >> 1;
  const int h = l >> 5, r32 = l & 31;
  const float scale = 0.26864248f;
  const float LOG2E = 1.44269504089f;
  const float c1 = scale * LOG2E, cn = -0.5f * scale * scale * LOG2E, c0 = -3.79248125036f;
  const size_t hoff = (size_t)head * 2048 * ROWB;
  STAMP(0);

  unsigned fa[4];
#pragma unroll
  for (int kk = 0; kk < 4; ++kk) fa[kk] = r32 * ROWB + ((((2 * kk + h) ^ ((r32 >> 1) & 7))) << 4);
  auto ldfrags = [&](u32x4* f, unsigned base) {
#pragma unroll
    for (int ks = 0; ks < 12; ++ks) f[ks] = ld128(L + base + fa[ks & 3] + (ks >> 2) * 128);
  };

  unsigned vo[3];
#pragma unroll
  for (int p = 0; p < 3; ++p) { int Lc = p * 64 + l, row = Lc / 24, c = Lc % 24; vo[p] = row * ROWB + ((c ^ (row >> 1)) << 4); }
  auto issue12 = [&](auto nt, unsigned dst, __amdgpu_buffer_rsrc_t srd, unsigned soff) {
#pragma unroll
    for (int a = 0; a < 4; ++a)
#pragma unroll
      for (int p = 0; p < 3; ++p) dma<decltype(nt)::v>(dst + a * 3072 + p * 1024, vo[p] ^ ((a & 1) << 6), srd, soff + a * 3072);
  };
  const __amdgpu_buffer_rsrc_t sQ = __builtin_amdgcn_make_buffer_rsrc((void*)(q + hoff + (size_t)i8 * 256 * ROWB), 0, 256 * ROWB, 0x00020000);
  u64 tprev = 0, acc_c = 0, acc_v = 0, acc_b = 0;
#define RCLOSE(r, waits) do { WAITL(); const u64 ta = TNOW(); waits; const u64 tb = TNOW(); BAR(); const u64 tc = TNOW(); \
    if (TS >= 2 && (r) >= 2 && (r) <= 11) { acc_c += ta - tprev; acc_v += tb - ta; acc_b += tc - tb; } tprev = tc; } while (0)
#define DUMPACC()

  if (w < 4) {
    const __amdgpu_buffer_rsrc_t sK = __builtin_amdgcn_make_buffer_rsrc((void*)(k + hoff + (size_t)sc * 512 * ROWB), 0, 512 * ROWB, 0x00020000);
    const __amdgpu_buffer_rsrc_t sP = __builtin_amdgcn_make_buffer_rsrc((void*)proj, 0, 192 * ROWB, 0x00020000);
    {
      const unsigned par = (w & 1) << 6;
#pragma unroll
      for (int t = 0; t < 3; ++t)
#pragma unroll
        for (int p = 0; p < 3; ++p) dma<0>(lbase + L_K + (3 + t) * TILE + w * 3072 + p * 1024, vo[p] ^ par, sP, (3 * mh + t) * TILE + w * 3072);
#pragma unroll
      for (int t = 0; t < 3; ++t)
#pragma unroll
        for (int p = 0; p < 3; ++p) dma<NT_IN>(lbase + L_K + t * TILE + w * 3072 + p * 1024, vo[p] ^ par, sK, t * TILE + w * 3072);
    }
    WAITV(6);
    BAR();
    u32x4 pf[12], kf[12];
    if (w < 3) {
      ldfrags(pf, L_K + (3 + w) * TILE);
      ldfrags(kf, L_K);
      WAITV(0);
      BAR();
      STAMP(1);
      unsigned xw[4];
#pragma unroll
      for (int qq = 0; qq < 4; ++qq) xw[qq] = L_X + r32 * 192 + w * 64 + ((((2 * qq + h) ^ ((r32 >> 1) & 7))) << 3);
      if (G1P) __builtin_amdgcn_s_setprio(G1P);
      int nslot = 1;
      f32x16 acc[2];
      float nb = 0.f, nbn = 0.f;
      auto g1_round = [&](auto par, auto first, auto last, int r) {
        constexpr int P = decltype(par)::v;
        constexpr bool F = decltype(first)::v, Z = decltype(last)::v;
        const unsigned kb = L_K + nslot * TILE;
        constexpr unsigned xb = (P ^ 1) * 6144;
        nslot = nslot == 6 ? 0 : nslot + 1;
        unsigned o[8];
        auto step = [&](auto ic) {
          constexpr int i = decltype(ic)::v;
          const f32x16& pa = acc[P ^ 1];
          o[i] = pk(__builtin_amdgcn_exp2f(__builtin_fmaf(pa[2 * i], c1, nb)), __builtin_amdgcn_exp2f(__builtin_fmaf(pa[2 * i + 1], c1, nb)));
          if constexpr (i & 1) { u32x2 t = {o[i - 1], o[i]}; *(LAS u32x2*)(L + xb + xw[i >> 1]) = t; }
        };
        SB();
        if constexpr (!F) sfor<0, Z ? 8 : NS>([&](auto ic) { step(ic); SB(); });
        if constexpr (!Z) {
          nbn = *(LAS float*)(L + L_BIAS + (r & 3) * 128 + r32 * 4);
          sfor<0, 12>([&](auto ic) {
            constexpr int ks = decltype(ic)::v;
            acc[P] = mfma(pf[ks], kf[ks], ks == 0 ? (f32x16){} : acc[P]);
            kf[ks] = ld128(L + kb + fa[ks & 3] + (ks >> 2) * 128);
            if constexpr (!F && (ks & 1) == 0 && NS + ks / 2 < 8) step(IC<NS + ks / 2>{});
            SB();
          });
          nb = nbn;
        }
      };
#define G1CLOSE() do { asm volatile("s_waitcnt lgkmcnt(%0)" ::"n"(NS >= 8 ? 13 : 11 - 2 * (7 - NS)) : "memory"); __builtin_amdgcn_s_barrier(); asm volatile("" ::: "memory"); } while (0)
      g1_round(IC<0>{}, IC<1>{}, IC<0>{}, 0);
      G1CLOSE();
#pragma unroll 1
      for (int r = 1; r < 15; r += 2) {
        if (TS && r == 5) STAMP(10);
        if (TS && r == 13) STAMP(11);
        g1_round(IC<1>{}, IC<0>{}, IC<0>{}, r);
        G1CLOSE();
        g1_round(IC<0>{}, IC<0>{}, IC<0>{}, r + 1);
        G1CLOSE();
      }
      g1_round(IC<1>{}, IC<0>{}, IC<0>{}, 15);
      G1CLOSE();
      g1_round(IC<0>{}, IC<0>{}, IC<1>{}, 16);
      BAR();
      if (G1P) __builtin_amdgcn_s_setprio(0);
    } else {
      float mk[16];
      {
        const int dg = (h == ((r32 >> 2) & 1)) ? 4 * (r32 >> 3) + (r32 & 3) : -1;
#pragma unroll
        for (int i = 0; i < 16; ++i) mk[i] = i == dg ? 1.f : 0.f;
      }
      auto putbias = [&](const f32x16& a, int slot) {
        float s0 = 0.f, s1 = 0.f;
#pragma unroll
        for (int i = 0; i < 8; ++i) { s0 = __builtin_fmaf(a[2 * i], mk[2 * i], s0); s1 = __builtin_fmaf(a[2 * i + 1], mk[2 * i + 1], s1); }
        const float n = halfsum(s0 + s1);
        if (l < 32) *(LAS float*)(L + L_BIAS + slot * 128 + l * 4) = __builtin_fmaf(n, cn, c0);
      };
      ldfrags(kf, L_K);
      {
        f32x16 a = {};
#pragma unroll
        for (int ks = 0; ks < 12; ++ks) a = mfma(kf[ks], kf[ks], a);
        putbias(a, 0);
      }
      WAITV(0);
      BAR();
      int islot = 6, nslot = 2;
#pragma unroll 1
      for (int j = 3; j < 6; ++j) issue12(IC<NT_IN>{}, lbase + L_K + j * TILE, sK, j * TILE);
      ldfrags(kf, L_K + TILE);
      u32x4 kg[12];
      auto w3_round = [&](auto nt, const u32x4* cur, u32x4* nxt, __amdgpu_buffer_rsrc_t srd, unsigned soff, int r) {
        const unsigned kb = L_K + nslot * TILE;
        nslot = nslot == 6 ? 0 : nslot + 1;
        const unsigned dst = lbase + L_K + islot * TILE;
        islot = islot == 6 ? 0 : islot + 1;
        f32x16 a;
        sfor<0, 12>([&](auto ic) {
          constexpr int n = decltype(ic)::v, aa = n / 3, p = n % 3;
          a = mfma(cur[n], cur[n], n == 0 ? (f32x16){} : a);
          nxt[n] = ld128(L + kb + fa[n & 3] + (n >> 2) * 128);
          dma<decltype(nt)::v>(dst + aa * 3072 + p * 1024, vo[p] ^ ((aa & 1) << 6), srd, soff + aa * 3072);
          SB();
        });
        putbias(a, (r + 1) & 3);
        RCLOSE(r, if (r <= 12) WAITV(36));
      };
#pragma unroll 1
      for (int r = 0; r < 10; r += 2) {
        w3_round(IC<NT_IN>{}, kf, kg, sK, (r + 6) * TILE, r);
        w3_round(IC<NT_IN>{}, kg, kf, sK, (r + 7) * TILE, r + 1);
      }
#pragma unroll 1
      for (int r = 10; r < 16; r += 2) {
        w3_round(IC<0>{}, kf, kg, sP, (r - 10) * TILE, r);
        w3_round(IC<0>{}, kg, kf, sP, (r - 9) * TILE, r + 1);
      }
      RCLOSE(16, );
      WAITV(0);
      if (l == 0) *(LAS volatile unsigned*)(L + L_FLG) = 1u;
    }
    DUMPACC();
    for (unsigned it = 0; it < SPIN_MAX; ++it) {
      const unsigned f0 = *(LAS volatile unsigned*)(L + L_FLG), f1 = *(LAS volatile unsigned*)(L + L_FLG + 4);
      if (f0 != 0u && f1 > (unsigned)w) break;
      __builtin_amdgcn_s_sleep(1);
    }
    asm volatile("" ::: "memory");
    STAMP(2);
    CSTAMP(14);
    unsigned pq[2][6][8];
    const unsigned qslot = L_V + (1 + w) * TILE;
#pragma unroll
    for (int t = 0; t < 2; ++t) {
      if (t == 1) {
        for (unsigned it = 0; it < SPIN_MAX; ++it) {
          if (*(LAS volatile unsigned*)(L + L_FLG + 4) > 4u + (unsigned)w) break;
          __builtin_amdgcn_s_sleep(1);
        }
        asm volatile("" ::: "memory");
      }
      ldfrags(kf, qslot);
      const u32x4* qf = kf;
      u32x4 A[6];
#pragma unroll
      for (int i = 0; i < 6; ++i) A[i] = ld128(L + L_K + 2 * TILE + fa[i & 3] + (i >> 2) * 128);
      WAITL();
      if (t == 0) *(LAS volatile unsigned*)(L + L_Q0 + w * 4) = 1u;
      else if (l == 0) __hip_atomic_fetch_add((LAS unsigned*)(L + L_CNT), 1u, __ATOMIC_RELAXED, __HIP_MEMORY_SCOPE_WORKGROUP);
      STAMP(6 + 2 * t);
      const float nb = __builtin_fmaf(rownorm(qf), cn, c0);
      f32x16 acc[2];
      SB();
      sfor<0, 84>([&](auto ic) {
        constexpr int n = decltype(ic)::v, u = n / 12, ks = n % 12;
        if constexpr (n < 72) {
          acc[u & 1] = mfma(A[n % 6], qf[ks], ks == 0 ? (f32x16){} : acc[u & 1]);
          if constexpr (n + 6 < 72) { constexpr int m = n + 6, uu = m / 12, kk = m % 12; A[n % 6] = ld128(L + L_K + ((2 + uu) % 7) * TILE + fa[kk & 3] + (kk >> 2) * 128); }
        }
        if constexpr (u >= 1 && ks >= 2 && ks < 10) {
          constexpr int i = ks - 2;
          const f32x16& pa = acc[(u - 1) & 1];
          pq[t][u - 1][i] = pk(__builtin_amdgcn_exp2f(__builtin_fmaf(pa[2 * i], c1, nb)), __builtin_amdgcn_exp2f(__builtin_fmaf(pa[2 * i + 1], c1, nb)));
          asm volatile("" : "+v"(pq[t][u - 1][i]));
        }
        SB();
      });
    }
    STAMP(3);
    CSTAMP(15);
    float inv[2];
    {
      for (unsigned it = 0; it < SPIN_MAX; ++it) {
        if (*(LAS volatile unsigned*)(L + L_KSC) == 3u) break;
        __builtin_amdgcn_s_sleep(1);
      }
      asm volatile("" ::: "memory");
      f32x16 d0 = {}, d1 = {};
#pragma unroll
      for (int us = 0; us < 12; ++us) {
        const u32x4 a = ld128(L + L_TAB + h * 16 + us * 32);
        const unsigned* p0 = &pq[0][us >> 1][(us & 1) * 4];
        const unsigned* p1 = &pq[1][us >> 1][(us & 1) * 4];
        d0 = mfma(a, (u32x4){p0[0], p0[1], p0[2], p0[3]}, d0);
        d1 = mfma(a, (u32x4){p1[0], p1[1], p1[2], p1[3]}, d1);
      }
      inv[0] = 1.0f / (d0[0] + 1e-6f); inv[1] = 1.0f / (d1[0] + 1e-6f);
      asm volatile("" : "+v"(inv[0]), "+v"(inv[1]));
    }
    STAMP(7);
    BAR();
    STAMP(4);
    const unsigned sa = w * 16384 + r32 * 128 + ((r32 & 7) << 4) + h * 8;
    {
      f32x16 acc[2][2];
      u32x4 A[6];
      const unsigned kb = L_V + l * 16;
      unsigned fr = 0;
#pragma unroll
      for (int i = 0; i < 6; ++i) A[i] = ld128(L + kb + i * 1024);
      SB();
      sfor<0, 82>([&](auto ic) {
        constexpr int n = decltype(ic)::v, c = n / 12, us = n % 12, pb = c & 1;
        if constexpr (n < 72) {
          const unsigned* p0 = &pq[0][us >> 1][(us & 1) * 4];
          const unsigned* p1 = &pq[1][us >> 1][(us & 1) * 4];
          acc[pb][0] = mfma(A[n % 6], (u32x4){p0[0], p0[1], p0[2], p0[3]}, us == 0 ? (f32x16){} : acc[pb][0]);
          acc[pb][1] = mfma(A[n % 6], (u32x4){p1[0], p1[1], p1[2], p1[3]}, us == 0 ? (f32x16){} : acc[pb][1]);
          if constexpr (n + 6 < 72) A[n % 6] = ld128(L + kb + (n + 6) * 1024);
        }
        if constexpr (n == 54) fr = *(LAS volatile unsigned*)(L + L_FR + w * 4);
        if constexpr (n == 60) {
          while (fr == 0u) fr = *(LAS volatile unsigned*)(L + L_FR + w * 4);
        }
        if constexpr (c >= 1 && us >= 1 && us <= 8) {
          constexpr int dt = c - 1, e = us - 1, t = e >> 2, qq = e & 3;
          const f32x16& a = acc[pb ^ 1][t];
          u32x2 o = {pk(a[4 * qq] * inv[t], a[4 * qq + 1] * inv[t]), pk(a[4 * qq + 2] * inv[t], a[4 * qq + 3] * inv[t])};
          *(LAS u32x2*)(L + (sa ^ ((((dt & 1) * 4 + qq)) << 4)) + ((dt >> 1) & 1) * 8192 + t * 4096) = o;
          if constexpr ((dt & 1) && us == 8) *(LAS volatile unsigned*)(L + L_SF + w * 4) = (dt >> 1) + 1;
        }
        SB();
      });
    }
    STAMP(5);
    __builtin_amdgcn_s_waitcnt(0);
    STAMP(9);
    DUMPTS();
    __builtin_amdgcn_s_waitcnt(0);
  } else {
    const int g = w - 4;
    char* const hello = ws + W_HEL + (size_t)head * 64;
    auto storer = [&]() {
      const __amdgpu_buffer_rsrc_t sO = __builtin_amdgcn_make_buffer_rsrc((void*)(out + hoff + (size_t)i8 * 256 * ROWB), 0, 256 * ROWB, 0x00020000);
      const unsigned sr = g * 16384 + (l >> 3) * 128 + (((l & 7) ^ ((l >> 3) & 7)) << 4);
      const unsigned so = (g * 64 + (l >> 3)) * ROWB + (l & 7) * 16;
#pragma unroll
      for (int cp = 0; cp < 3; ++cp) {
        for (unsigned it = 0; it < SPIN_MAX; ++it) {
          if (*(LAS volatile unsigned*)(L + L_SF + g * 4) > (unsigned)cp) break;
          __builtin_amdgcn_s_sleep(2);
        }
        asm volatile("" ::: "memory");
        u32x4 val[8];
#pragma unroll
        for (int i = 0; i < 8; ++i) val[i] = ld128(L + sr + (cp & 1) * 8192 + i * 1024);
        WAITL();
        if (cp == 0) *(LAS volatile unsigned*)(L + L_FR + g * 4) = 1u;
#pragma unroll
        for (int i = 0; i < 8; ++i) __builtin_amdgcn_raw_buffer_store_b128(val[i], sO, so + i * 8 * ROWB + cp * 128, 0, NT_OUT);
      }
      __builtin_amdgcn_s_waitcnt(0);
      STAMP(9);
    };
    if (w == 7) {
      unsigned vv[3];
#pragma unroll
      for (int p = 0; p < 3; ++p) { int Lc = p * 64 + l, row = Lc / 24, c = Lc % 24; vv[p] = row * ROWB + ((c ^ (((row >> 1) & 1) << 2)) << 4); }
      const __amdgpu_buffer_rsrc_t sV = __builtin_amdgcn_make_buffer_rsrc((void*)(v + hoff + (size_t)sc * 512 * ROWB), 0, 512 * ROWB, 0x00020000);
      auto issueV = [&](int j, int slot) {
#pragma unroll
        for (int a = 0; a < 4; ++a)
#pragma unroll
          for (int p = 0; p < 3; ++p) dma<NT_IN>(lbase + L_V + slot * TILE + a * 3072 + p * 1024, vv[p], sV, j * TILE + a * 3072);
      };
      const u64 old = ald(hello + i8 * 8);
      const u64 n = old * 0x9E3779B97F4A7C15ull + 0x632BE59BD9B4E019ull;
      if (l == 0) {
        ast(hello + i8 * 8, n);
        *(LAS u64*)(L + L_NON) = n; *(LAS unsigned*)(L + L_CNT) = 0u; *(LAS u64*)(L + L_FLG) = 0ull; *(LAS unsigned*)(L + L_KSC) = 0u;
        *(LAS u32x4*)(L + L_SF) = (u32x4){0u, 0u, 0u, 0u}; *(LAS u32x4*)(L + L_FR) = (u32x4){0u, 0u, 0u, 0u};
        *(LAS u32x4*)(L + L_Q0) = (u32x4){0u, 0u, 0u, 0u}; *(LAS unsigned*)(L + L_DRN) = 0u;
      }
#pragma unroll 1
      for (int j = 0; j < 3; ++j) issueV(j, j);
      BAR();
      BAR();
      int islot = 2;
#pragma unroll 1
      for (int r = 0; r <= 16; ++r) {
        if (r >= 1 && r <= 13) issueV(r + 2, islot);
        else if (r >= 14) issue12(IC<NT_IN>{}, lbase + L_V + (r - 13) * TILE, sQ, 2 * (r - 14) * TILE);
        islot = islot == 4 ? 0 : islot + 1;
        RCLOSE(r, WAITV(36));
      }
      issue12(IC<NT_IN>{}, lbase + L_V + 4 * TILE, sQ, 6 * TILE);
      DUMPACC();
      WAITV(36); if (l == 0) *(LAS volatile unsigned*)(L + L_FLG + 4) = 1u;
      WAITV(24); if (l == 0) *(LAS volatile unsigned*)(L + L_FLG + 4) = 2u;
      WAITV(12); if (l == 0) *(LAS volatile unsigned*)(L + L_FLG + 4) = 3u;
      WAITV(0);  if (l == 0) *(LAS volatile unsigned*)(L + L_FLG + 4) = 4u;
      if (DRAINW) {
        for (unsigned it = 0; it < SPIN_MAX; ++it) {
          if (*(LAS volatile unsigned*)(L + L_DRN) >= DRAINW) break;
          __builtin_amdgcn_s_sleep(1);
        }
      }
#pragma unroll 1
      for (int j = 0; j < 4; ++j) {
        for (unsigned it = 0; it < SPIN_MAX; ++it) {
          if (*(LAS volatile unsigned*)(L + L_Q0 + j * 4) != 0u) break;
          __builtin_amdgcn_s_sleep(1);
        }
        asm volatile("" ::: "memory");
        issue12(IC<NT_IN>{}, lbase + L_V + (1 + j) * TILE, sQ, (2 * j + 1) * TILE);
      }
      WAITV(36); if (l == 0) *(LAS volatile unsigned*)(L + L_FLG + 4) = 5u;
      WAITV(24); if (l == 0) *(LAS volatile unsigned*)(L + L_FLG + 4) = 6u;
      WAITV(12); if (l == 0) *(LAS volatile unsigned*)(L + L_FLG + 4) = 7u;
      WAITV(0);  if (l == 0) *(LAS volatile unsigned*)(L + L_FLG + 4) = 8u;
      BAR();
      storer();
    } else {
      BAR();
      BAR();
      const int blk = (l >> 4) & 1, q4 = (l & 15) >> 2, p4 = l & 3;
      unsigned xa[2], va[2];
#pragma unroll
      for (int t = 0; t < 2; ++t) xa[t] = L_X + (8 * h + 4 * t + q4) * 192 + ((((4 * blk + p4) ^ (4 * h + 2 * t + (q4 >> 1)))) << 3);
#pragma unroll
      for (int e = 0; e < 2; ++e) va[e] = L_V + (8 * h + q4) * ROWB + (2 * g + (e ^ ((q4 >> 1) & 1))) * 64 + blk * 32 + p4 * 8;
      f32x16 acc[2][3] = {};
      float ksum = 0.f, ksum2 = 0.f;
      u64 view = 0;
      u32x4 B[2][2], A[2][2][3];
      auto ldB = [&](unsigned vb) {
#pragma unroll
        for (int ks = 0; ks < 2; ++ks)
#pragma unroll
          for (int e = 0; e < 2; ++e) {
            const unsigned ad = va[e] + vb;
            u32x2 b0 = ldtr(L + ad + ks * 6144), b1 = ldtr(L + ad + ks * 6144 + 1536);
            B[ks][e] = (u32x4){b0[0], b0[1], b1[0], b1[1]};
          }
      };
      int vslot = 0;
      auto g2_round = [&](auto par, auto domul, auto doread) {
        constexpr int P = decltype(par)::v;
        constexpr bool M = decltype(domul)::v, R = decltype(doread)::v;
        SB();
        sfor<0, 12>([&](auto ic) {
          constexpr int n = decltype(ic)::v, ks = n / 6, j = (n % 6) >> 1, e = n & 1;
          if constexpr (M) acc[e][j] = mfma(A[P ^ 1][ks][j], B[ks][e], acc[e][j]);
          if constexpr (R && n < 6) {
            u32x2 a0 = ldtr(L + xa[0] + P * 6144 + (n / 3) * 3072 + (n % 3) * 64), a1 = ldtr(L + xa[1] + P * 6144 + (n / 3) * 3072 + (n % 3) * 64);
            A[P][n / 3][n % 3] = (u32x4){a0[0], a0[1], a1[0], a1[1]};
          }
          SB();
        });
        if constexpr (R) { ldB(vslot * TILE); vslot = vslot == 4 ? 0 : vslot + 1; }
        if constexpr (M) {
          auto sum = [&](auto jc) {
            constexpr int j = decltype(jc)::v;
#pragma unroll
            for (int ks = 0; ks < 2; ++ks)
#pragma unroll
              for (int i = 0; i < 4; ++i) {
                if (KSV) { ksum += bflo(A[P ^ 1][ks][j][i]); ksum2 += bfhi(A[P ^ 1][ks][j][i]); }
                else ksum = dot2(A[P ^ 1][ks][j][i], 0x3f803f80u, ksum);
              }
            asm volatile("" : "+v"(ksum), "+v"(ksum2));
          };
          if (g == 0) sum(IC<0>{}); else if (g == 1) sum(IC<1>{}); else sum(IC<2>{});
        }
      };
      if (G1PRIO) __builtin_amdgcn_s_setprio(G1PRIO);
      RCLOSE(0, );
      RCLOSE(1, );
      g2_round(IC<0>{}, IC<0>{}, IC<1>{});
      RCLOSE(2, );
#pragma unroll 1
      for (int r = 3; r <= 15; r += 2) {
        g2_round(IC<1>{}, IC<1>{}, IC<1>{});
        RCLOSE(r, );
        g2_round(IC<0>{}, IC<1>{}, IC<1>{});
        RCLOSE(r + 1, );
      }
      view = ald(hello + (l & 7) * 8);
      g2_round(IC<1>{}, IC<1>{}, IC<1>{});
      g2_round(IC<0>{}, IC<1>{}, IC<0>{});
      DUMPACC();
      if (G1PRIO) __builtin_amdgcn_s_setprio(0);
      STAMP(2);
      WAITL();
      if (l == 0) __hip_atomic_fetch_add((LAS unsigned*)(L + L_CNT), 1u, __ATOMIC_RELAXED, __HIP_MEMORY_SCOPE_WORKGROUP);
      const __amdgpu_buffer_rsrc_t sW = __builtin_amdgcn_make_buffer_rsrc((void*)ws, 0, (int)W_INB, 0x00020000);
      const unsigned p1own = (unsigned)W_P1 + (unsigned)((((head * 2 + mh) * 4 + sc) * 3 + g) * 12) * 1024u;
      float own[3][8];
#pragma unroll
      for (int f = 0; f < 12; ++f) {
        const f32x16& a = acc[f / 6][(f % 6) >> 1];
        const int o = (f & 1) * 8;
        if (f / 3 != sc) {
          u32x4 val = {pk(a[o], a[o + 1]), pk(a[o + 2], a[o + 3]), pk(a[o + 4], a[o + 5]), pk(a[o + 6], a[o + 7])};
          __builtin_amdgcn_raw_buffer_store_b128(val, sW, l * 16 + p1own + f * 1024, 0, 16);
        } else {
#pragma unroll
          for (int i = 0; i < 8; ++i) own[f % 3][i] = a[o + i];
        }
      }
      {
        const float ks = halfsum(ksum + ksum2);
        if (l < 32) __builtin_amdgcn_raw_buffer_store_b32(__float_as_uint(ks), sW, l * 4, (unsigned)W_KS + ((head * 8 + i8) * 3 + g) * 128, 16);
      }
      const u64 nown = *(LAS u64*)(L + L_NON);
      char* const inb = ws + W_INB;
      char* const myin = inb + ((size_t)(head * 8 + i8) * 3 + g) * 128;
      char* const tgt = inb + ((size_t)(head * 8 + (l & 7)) * 3 + g) * 128;
      const bool pub = l >= 8 && l < 16;
      WAITV(0);
      if (pub) ast(tgt + i8 * 8, view);
      if (DRAINW && l == 0) __hip_atomic_fetch_add((LAS unsigned*)(L + L_DRN), 1u, __ATOMIC_RELAXED, __HIP_MEMORY_SCOPE_WORKGROUP);
      STAMP(3);
      {
        const char* pa = pub ? hello + (l & 7) * 8 : myin + (l & 7) * 8;
        for (unsigned it = 0; it < SPIN_MAX; ++it) {
          const u64 x = ald(pa);
          if (pub && x != view) { view = x; ast(tgt + i8 * 8, view); }
          const bool ok = l >= 8 || x == nown;
          if (__all(ok)) break;
          __builtin_amdgcn_s_sleep(POLL_SLEEP);
        }
      }
      asm volatile("" ::: "memory");
      STAMP(4);
      u32x4 pin[3][3];
#pragma unroll
      for (int s = 0; s < 3; ++s) {
        const int so = s + (s >= sc);
        const unsigned base = (unsigned)W_P1 + (unsigned)(((((head * 2 + mh) * 4 + so) * 3 + g) * 12) + 3 * sc) * 1024u;
#pragma unroll
        for (int i = 0; i < 3; ++i) pin[s][i] = __builtin_amdgcn_raw_buffer_load_b128(sW, l * 16, base + i * 1024, 16);
      }
      float kst = 0.f;
#pragma unroll
      for (int s = 0; s < 4; ++s)
        kst += __uint_as_float(__builtin_amdgcn_raw_buffer_load_b32(sW, h * 384 + r32 * 4, (unsigned)W_KS + ((head * 8 + 2 * s) * 3 + g) * 128, 16));
      {
        const u64 x = ald(hello + (l & 7) * 8);
        if (pub && x != view) { view = x; ast(tgt + i8 * 8, view); }
      }
      {
        const int u = 3 * h + g;
        const unsigned off = ((u * 2 + (r32 >> 4)) * 2 + ((r32 >> 2) & 1)) * 16 + (((r32 >> 3) & 1) * 4 + (r32 & 3)) * 2;
        *(LAS unsigned short*)(L + L_TAB + off) = (unsigned short)(pk(kst, 0.f) & 0xffffu);
        if (l == 0) __hip_atomic_fetch_add((LAS unsigned*)(L + L_KSC), 1u, __ATOMIC_RELAXED, __HIP_MEMORY_SCOPE_WORKGROUP);
      }
#pragma unroll
      for (int i = 0; i < 3; ++i) {
#pragma unroll
        for (int s = 0; s < 3; ++s)
#pragma unroll
          for (int e = 0; e < 4; ++e) { own[i][2 * e] += bflo(pin[s][i][e]); own[i][2 * e + 1] += bfhi(pin[s][i][e]); }
        const int f = 3 * sc + i;
        const int cc = 2 * g + f / 6, uu = 3 * mh + ((f % 6) >> 1), sp = f & 1;
        u32x4 val = {pk(own[i][0], own[i][1]), pk(own[i][2], own[i][3]), pk(own[i][4], own[i][5]), pk(own[i][6], own[i][7])};
        __builtin_amdgcn_raw_buffer_store_b128(val, sW, l * 16 + (unsigned)W_FIN + (unsigned)(head * 72 + (cc * 6 + uu) * 2 + sp) * 1024u, 0, 16);
      }
      WAITV(0);
      if (pub) ast(tgt + 64 + i8 * 8, view);
      STAMP(5);
      {
        const char* pa = pub ? hello + (l & 7) * 8 : myin + 64 + (l & 7) * 8;
        for (unsigned it = 0; it < SPIN_MAX; ++it) {
          const u64 x = ald(pa);
          if (pub && x != view) { view = x; ast(tgt + 64 + i8 * 8, view); }
          const bool ok = l >= 8 || x == nown;
          if (__all(ok)) break;
          __builtin_amdgcn_s_sleep(POLL_SLEEP);
        }
      }
      asm volatile("" ::: "memory");
      STAMP(6);
      u32x4 fin[24];
      const unsigned fb = (unsigned)W_FIN + (unsigned)(head * 72 + g * 24) * 1024u;
#pragma unroll
      for (int i = 0; i < 24; ++i) fin[i] = __builtin_amdgcn_raw_buffer_load_b128(sW, l * 16, fb + i * 1024, 16);
      {
        const u64 x = ald(hello + (l & 7) * 8);
        if (pub && x != view) { view = x; ast(tgt + 64 + i8 * 8, view); }
      }
      for (unsigned it = 0; it < SPIN_MAX; ++it) {
        if (__hip_atomic_load((LAS unsigned*)(L + L_CNT), __ATOMIC_RELAXED, __HIP_MEMORY_SCOPE_WORKGROUP) == 7u) break;
        __builtin_amdgcn_s_sleep(1);
      }
      asm volatile("" ::: "memory");
#pragma unroll
      for (int i = 0; i < 24; ++i) *(LAS u32x4*)(L + L_V + (g * 24 + i) * 1024 + l * 16) = fin[i];
      STAMP(7);
      BAR();
      storer();
    }
    DUMPTS();
    __builtin_amdgcn_s_waitcnt(0);
  }
}

extern "C" void kernel_launch(void* const* d_in, const int* in_sizes, int n_in, void* d_out, int out_size, void* d_ws, size_t ws_size,
                              hipStream_t stream) {
  performer_fused<<<256, 512, 0, stream>>>((const char*)d_in[0], (const char*)d_in[1], (const char*)d_in[2], (const char*)d_in[3], (char*)d_out,
                                           (char*)d_ws);
}
```

```cpp
#include <hip/hip_runtime.h>
#include <hip/hip_bf16.h>
#include <stdint.h>

typedef unsigned long long u64;
typedef __attribute__((ext_vector_type(8))) __bf16 bf16x8;
typedef __attribute__((ext_vector_type(2))) __bf16 bf16x2;
typedef __attribute__((ext_vector_type(2))) float f32x2;
typedef __attribute__((ext_vector_type(16))) float f32x16;
typedef __attribute__((ext_vector_type(4))) unsigned u32x4;
typedef __attribute__((ext_vector_type(2))) unsigned u32x2;
typedef __attribute__((ext_vector_type(4))) short s16x4;
#define LAS __attribute__((address_space(3)))
typedef LAS char* lptr;

#define NT_IN 1
#define NT_OUT 19
#define TS 0

constexpr int ROWB = 384;
constexpr int TILE = 12288;
constexpr int L_K = 0, L_V = 7 * TILE, L_X = L_V + 5 * TILE, L_BIAS = L_X + TILE, L_TAB = L_BIAS + 512, L_CNT = L_TAB + 512, L_NON = L_CNT + 8,
              L_FLG = L_CNT + 16, L_KSC = L_CNT + 32, L_SF = L_CNT + 48, L_FR = L_CNT + 64, L_Q0 = L_CNT + 80, L_DRN = L_CNT + 96, L_END = L_CNT + 112;
constexpr size_t W_P1 = 0;
constexpr size_t W_FIN = 32ull * 2 * 4 * 3 * 12 * 1024;
constexpr size_t W_KS = W_FIN + 32ull * 72 * 1024;
constexpr size_t W_INB = W_KS + 32ull * 8 * 3 * 128;
constexpr size_t W_HEL = W_INB + 256ull * 3 * 128;
constexpr size_t W_TS = W_HEL + 4096;
constexpr unsigned SPIN_MAX = 1u << 22;

__device__ __forceinline__ f32x16 mfma(u32x4 a, u32x4 b, f32x16 c) {
  return __builtin_amdgcn_mfma_f32_32x32x16_bf16(__builtin_bit_cast(bf16x8, a), __builtin_bit_cast(bf16x8, b), c, 0, 0, 0);
}
__device__ __forceinline__ unsigned pk(float a, float b) {
  f32x2 v = {a, b};
  return __builtin_bit_cast(unsigned, __builtin_convertvector(v, bf16x2));
}
__device__ __forceinline__ float dot2(unsigned a, unsigned b, float c) {
  return __builtin_amdgcn_fdot2_f32_bf16(__builtin_bit_cast(bf16x2, a), __builtin_bit_cast(bf16x2, b), c, false);
}
__device__ __forceinline__ u32x4 ld128(lptr p) { return *(LAS u32x4*)p; }
__device__ __forceinline__ u32x2 ldtr(lptr p) { return __builtin_bit_cast(u32x2, __builtin_amdgcn_ds_read_tr16_b64_v4i16((LAS s16x4*)p)); }
__device__ __forceinline__ float bflo(unsigned x) { return __uint_as_float(x << 16); }
__device__ __forceinline__ float bfhi(unsigned x) { return __uint_as_float(x & 0xffff0000u); }
__device__ __forceinline__ float halfsum(float x) {
  auto r = __builtin_amdgcn_permlane32_swap(__float_as_uint(x), __float_as_uint(x), false, false);
  return __uint_as_float(r[0]) + __uint_as_float(r[1]);
}
template <int NT>
__device__ __forceinline__ void dma(unsigned ldsaddr, unsigned voff, __amdgpu_buffer_rsrc_t srd, unsigned soff) {
  if constexpr (NT)
    asm volatile("s_mov_b32 m0, %0\n\ts_nop 0\n\tbuffer_load_dwordx4 %1, %2, %3 offen nt lds" ::"s"(ldsaddr), "v"(voff), "s"(srd), "s"(soff) : "memory");
  else
    asm volatile("s_mov_b32 m0, %0\n\ts_nop 0\n\tbuffer_load_dwordx4 %1, %2, %3 offen lds" ::"s"(ldsaddr), "v"(voff), "s"(srd), "s"(soff) : "memory");
}
template <int V> struct IC { static constexpr int v = V; };
template <int I, int N, class F> __device__ __forceinline__ void sfor(F&& f) { if constexpr (I < N) { f(IC<I>{}); sfor<I + 1, N>(f); } }
#define SB() __builtin_amdgcn_sched_barrier(0)
#define G1PRIO 1
#define Q1AUX 0
#define DRAINW 3u
#define G1P 0
#define NS 8
#define KSV 1
#define QR0 3
#define QRN 6
#define POLL_SLEEP 2
#define PUBR 13
constexpr unsigned EMPTY = 0xffffffffu;
__device__ __forceinline__ unsigned ne(unsigned x) { return x < EMPTY - 1u ? x : EMPTY - 1u; }
__device__ __forceinline__ unsigned max3u(unsigned a, unsigned b, unsigned c) { const unsigned m = a > b ? a : b; return m > c ? m : c; }
#define WAITV(n) asm volatile("s_waitcnt vmcnt(" #n ")" ::: "memory")
#define WAITL() asm volatile("s_waitcnt lgkmcnt(0)" ::: "memory")
#define BAR() do { asm volatile("s_waitcnt lgkmcnt(0)" ::: "memory"); __builtin_amdgcn_s_barrier(); asm volatile("" ::: "memory"); } while (0)
typedef __attribute__((address_space(1))) u64 gu64;
__device__ __forceinline__ u64 ald(const char* p) { return __hip_atomic_load((gu64*)p, __ATOMIC_RELAXED, __HIP_MEMORY_SCOPE_AGENT); }
__device__ __forceinline__ void ast(char* p, u64 v) { __hip_atomic_store((gu64*)p, v, __ATOMIC_RELAXED, __HIP_MEMORY_SCOPE_AGENT); }
#define STAMP(i)
#define CSTAMP(i)
#define DUMPTS()
#define TNOW() 0ull

__device__ __forceinline__ void feat(const f32x16& acc, float c1, float nb, unsigned* o) {
#pragma unroll
  for (int i = 0; i < 8; ++i)
    o[i] = pk(__builtin_amdgcn_exp2f(__builtin_fmaf(acc[2 * i], c1, nb)), __builtin_amdgcn_exp2f(__builtin_fmaf(acc[2 * i + 1], c1, nb)));
}
__device__ __forceinline__ float rownorm(const u32x4* f) {
  float s0 = 0.f, s1 = 0.f;
#pragma unroll
  for (int i = 0; i < 12; ++i) {
    s0 = dot2(f[i][0], f[i][0], s0); s1 = dot2(f[i][1], f[i][1], s1);
    s0 = dot2(f[i][2], f[i][2], s0); s1 = dot2(f[i][3], f[i][3], s1);
  }
  return halfsum(s0 + s1);
}

__global__ __launch_bounds__(512, 2) void performer_fused(const char* __restrict__ q, const char* __restrict__ k, const char* __restrict__ v,
                                                          const char* __restrict__ proj, char* __restrict__ out, char* __restrict__ ws) {
  __shared__ __attribute__((aligned(16))) char lds_raw[L_END + (TS ? 1024 : 0)];
  lptr L = (lptr)lds_raw;
  const unsigned lbase = (unsigned)(uintptr_t)L;
  const int b = blockIdx.x;
  const int l = threadIdx.x & 63;
  const int w = __builtin_amdgcn_readfirstlane(threadIdx.x >> 6);
  const int head = (b & 7) * 4 + (b >> 6), i8 = (b >> 3) & 7, mh = i8 & 1, sc = i8 >> 1;
  const int h = l >> 5, r32 = l & 31;
  const float scale = 0.26864248f;
  const float LOG2E = 1.44269504089f;
  const float c1 = scale * LOG2E, cn = -0.5f * scale * scale * LOG2E, c0 = -3.79248125036f;
  const size_t hoff = (size_t)head * 2048 * ROWB;
  STAMP(0);

  unsigned fa[4];
#pragma unroll
  for (int kk = 0; kk < 4; ++kk) fa[kk] = r32 * ROWB + ((((2 * kk + h) ^ ((r32 >> 1) & 7))) << 4);
  auto ldfrags = [&](u32x4* f, unsigned base) {
#pragma unroll
    for (int ks = 0; ks < 12; ++ks) f[ks] = ld128(L + base + fa[ks & 3] + (ks >> 2) * 128);
  };

  unsigned vo[3];
#pragma unroll
  for (int p = 0; p < 3; ++p) { int Lc = p * 64 + l, row = Lc / 24, c = Lc % 24; vo[p] = row * ROWB + ((c ^ (row >> 1)) << 4); }
  auto issue12 = [&](auto nt, unsigned dst, __amdgpu_buffer_rsrc_t srd, unsigned soff) {
#pragma unroll
    for (int a = 0; a < 4; ++a)
#pragma unroll
      for (int p = 0; p < 3; ++p) dma<decltype(nt)::v>(dst + a * 3072 + p * 1024, vo[p] ^ ((a & 1) << 6), srd, soff + a * 3072);
  };
  const __amdgpu_buffer_rsrc_t sQ = __builtin_amdgcn_make_buffer_rsrc((void*)(q + hoff + (size_t)i8 * 256 * ROWB), 0, 256 * ROWB, 0x00020000);
  u64 tprev = 0, acc_c = 0, acc_v = 0, acc_b = 0;
#define RCLOSE(r, waits) do { WAITL(); const u64 ta = TNOW(); waits; const u64 tb = TNOW(); BAR(); const u64 tc = TNOW(); \
    if (TS >= 2 && (r) >= 2 && (r) <= 11) { acc_c += ta - tprev; acc_v += tb - ta; acc_b += tc - tb; } tprev = tc; } while (0)
#define DUMPACC()

  if (w < 4) {
    const __amdgpu_buffer_rsrc_t sK = __builtin_amdgcn_make_buffer_rsrc((void*)(k + hoff + (size_t)sc * 512 * ROWB), 0, 512 * ROWB, 0x00020000);
    const __amdgpu_buffer_rsrc_t sP = __builtin_amdgcn_make_buffer_rsrc((void*)proj, 0, 192 * ROWB, 0x00020000);
    {
      const unsigned par = (w & 1) << 6;
#pragma unroll
      for (int t = 0; t < 3; ++t)
#pragma unroll
        for (int p = 0; p < 3; ++p) dma<0>(lbase + L_K + (3 + t) * TILE + w * 3072 + p * 1024, vo[p] ^ par, sP, (3 * mh + t) * TILE + w * 3072);
#pragma unroll
      for (int t = 0; t < 3; ++t)
#pragma unroll
        for (int p = 0; p < 3; ++p) dma<NT_IN>(lbase + L_K + t * TILE + w * 3072 + p * 1024, vo[p] ^ par, sK, t * TILE + w * 3072);
    }
    WAITV(6);
    BAR();
    u32x4 pf[12], kf[12];
    if (w < 3) {
      ldfrags(pf, L_K + (3 + w) * TILE);
      ldfrags(kf, L_K);
      WAITV(0);
      BAR();
      STAMP(1);
      unsigned xw[4];
#pragma unroll
      for (int qq = 0; qq < 4; ++qq) xw[qq] = L_X + r32 * 192 + w * 64 + ((((2 * qq + h) ^ ((r32 >> 1) & 7))) << 3);
      if (G1P) __builtin_amdgcn_s_setprio(G1P);
      int nslot = 1;
      f32x16 acc[2];
      float nb = 0.f, nbn = 0.f;
      auto g1_round = [&](auto par, auto first, auto last, int r) {
        constexpr int P = decltype(par)::v;
        constexpr bool F = decltype(first)::v, Z = decltype(last)::v;
        const unsigned kb = L_K + nslot * TILE;
        constexpr unsigned xb = (P ^ 1) * 6144;
        nslot = nslot == 6 ? 0 : nslot + 1;
        unsigned o[8];
        auto step = [&](auto ic) {
          constexpr int i = decltype(ic)::v;
          const f32x16& pa = acc[P ^ 1];
          o[i] = pk(__builtin_amdgcn_exp2f(__builtin_fmaf(pa[2 * i], c1, nb)), __builtin_amdgcn_exp2f(__builtin_fmaf(pa[2 * i + 1], c1, nb)));
          if constexpr (i & 1) { u32x2 t = {o[i - 1], o[i]}; *(LAS u32x2*)(L + xb + xw[i >> 1]) = t; }
        };
        SB();
        if constexpr (!F) sfor<0, Z ? 8 : NS>([&](auto ic) { step(ic); SB(); });
        if constexpr (!Z) {
          nbn = *(LAS float*)(L + L_BIAS + (r & 3) * 128 + r32 * 4);
          sfor<0, 12>([&](auto ic) {
            constexpr int ks = decltype(ic)::v;
            acc[P] = mfma(pf[ks], kf[ks], ks == 0 ? (f32x16){} : acc[P]);
            kf[ks] = ld128(L + kb + fa[ks & 3] + (ks >> 2) * 128);
            if constexpr (!F && (ks & 1) == 0 && NS + ks / 2 < 8) step(IC<NS + ks / 2>{});
            SB();
          });
          nb = nbn;
        }
      };
#define G1CLOSE() do { asm volatile("s_waitcnt lgkmcnt(%0)" ::"n"(NS >= 8 ? 13 : 11 - 2 * (7 - NS)) : "memory"); __builtin_amdgcn_s_barrier(); asm volatile("" ::: "memory"); } while (0)
      g1_round(IC<0>{}, IC<1>{}, IC<0>{}, 0);
      G1CLOSE();
#pragma unroll 1
      for (int r = 1; r < 15; r += 2) {
        if (TS && r == 5) STAMP(10);
        if (TS && r == 13) STAMP(11);
        g1_round(IC<1>{}, IC<0>{}, IC<0>{}, r);
        G1CLOSE();
        g1_round(IC<0>{}, IC<0>{}, IC<0>{}, r + 1);
        G1CLOSE();
      }
      g1_round(IC<1>{}, IC<0>{}, IC<0>{}, 15);
      G1CLOSE();
      g1_round(IC<0>{}, IC<0>{}, IC<1>{}, 16);
      BAR();
      if (G1P) __builtin_amdgcn_s_setprio(0);
    } else {
      float mk[16];
      {
        const int dg = (h == ((r32 >> 2) & 1)) ? 4 * (r32 >> 3) + (r32 & 3) : -1;
#pragma unroll
        for (int i = 0; i < 16; ++i) mk[i] = i == dg ? 1.f : 0.f;
      }
      auto putbias = [&](const f32x16& a, int slot) {
        float s0 = 0.f, s1 = 0.f;
#pragma unroll
        for (int i = 0; i < 8; ++i) { s0 = __builtin_fmaf(a[2 * i], mk[2 * i], s0); s1 = __builtin_fmaf(a[2 * i + 1], mk[2 * i + 1], s1); }
        const float n = halfsum(s0 + s1);
        if (l < 32) *(LAS float*)(L + L_BIAS + slot * 128 + l * 4) = __builtin_fmaf(n, cn, c0);
      };
      ldfrags(kf, L_K);
      {
        f32x16 a = {};
#pragma unroll
        for (int ks = 0; ks < 12; ++ks) a = mfma(kf[ks], kf[ks], a);
        putbias(a, 0);
      }
      WAITV(0);
      BAR();
      int islot = 6, nslot = 2;
#pragma unroll 1
      for (int j = 3; j < 6; ++j) issue12(IC<NT_IN>{}, lbase + L_K + j * TILE, sK, j * TILE);
      ldfrags(kf, L_K + TILE);
      u32x4 kg[12];
      auto w3_round = [&](auto nt, const u32x4* cur, u32x4* nxt, __amdgpu_buffer_rsrc_t srd, unsigned soff, int r) {
        const unsigned kb = L_K + nslot * TILE;
        nslot = nslot == 6 ? 0 : nslot + 1;
        const unsigned dst = lbase + L_K + islot * TILE;
        islot = islot == 6 ? 0 : islot + 1;
        f32x16 a;
        sfor<0, 12>([&](auto ic) {
          constexpr int n = decltype(ic)::v, aa = n / 3, p = n % 3;
          a = mfma(cur[n], cur[n], n == 0 ? (f32x16){} : a);
          nxt[n] = ld128(L + kb + fa[n & 3] + (n >> 2) * 128);
          dma<decltype(nt)::v>(dst + aa * 3072 + p * 1024, vo[p] ^ ((aa & 1) << 6), srd, soff + aa * 3072);
          SB();
        });
        putbias(a, (r + 1) & 3);
        RCLOSE(r, if (r <= 12) WAITV(36));
      };
#pragma unroll 1
      for (int r = 0; r < 10; r += 2) {
        w3_round(IC<NT_IN>{}, kf, kg, sK, (r + 6) * TILE, r);
        w3_round(IC<NT_IN>{}, kg, kf, sK, (r + 7) * TILE, r + 1);
      }
#pragma unroll 1
      for (int r = 10; r < 16; r += 2) {
        w3_round(IC<0>{}, kf, kg, sP, (r - 10) * TILE, r);
        w3_round(IC<0>{}, kg, kf, sP, (r - 9) * TILE, r + 1);
      }
      RCLOSE(16, );
      WAITV(0);
      if (l == 0) *(LAS volatile unsigned*)(L + L_FLG) = 1u;
    }
    DUMPACC();
    for (unsigned it = 0; it < SPIN_MAX; ++it) {
      const unsigned f0 = *(LAS volatile unsigned*)(L + L_FLG), f1 = *(LAS volatile unsigned*)(L + L_FLG + 4);
      if (f0 != 0u && f1 > (unsigned)w) break;
      __builtin_amdgcn_s_sleep(1);
    }
    asm volatile("" ::: "memory");
    STAMP(2);
    CSTAMP(14);
    unsigned pq[2][6][8];
    const unsigned qslot = L_V + (1 + w) * TILE;
#pragma unroll
    for (int t = 0; t < 2; ++t) {
      if (t == 1) {
        for (unsigned it = 0; it < SPIN_MAX; ++it) {
          if (*(LAS volatile unsigned*)(L + L_FLG + 4) > 4u + (unsigned)w) break;
          __builtin_amdgcn_s_sleep(1);
        }
        asm volatile("" ::: "memory");
      }
      ldfrags(kf, qslot);
      const u32x4* qf = kf;
      u32x4 A[6];
#pragma unroll
      for (int i = 0; i < 6; ++i) A[i] = ld128(L + L_K + 2 * TILE + fa[i & 3] + (i >> 2) * 128);
      WAITL();
      if (t == 0) *(LAS volatile unsigned*)(L + L_Q0 + w * 4) = 1u;
      else if (l == 0) __hip_atomic_fetch_add((LAS unsigned*)(L + L_CNT), 1u, __ATOMIC_RELAXED, __HIP_MEMORY_SCOPE_WORKGROUP);
      STAMP(6 + 2 * t);
      const float nb = __builtin_fmaf(rownorm(qf), cn, c0);
      f32x16 acc[2];
      SB();
      sfor<0, 84>([&](auto ic) {
        constexpr int n = decltype(ic)::v, u = n / 12, ks = n % 12;
        if constexpr (n < 72) {
          acc[u & 1] = mfma(A[n % 6], qf[ks], ks == 0 ? (f32x16){} : acc[u & 1]);
          if constexpr (n + 6 < 72) { constexpr int m = n + 6, uu = m / 12, kk = m % 12; A[n % 6] = ld128(L + L_K + ((2 + uu) % 7) * TILE + fa[kk & 3] + (kk >> 2) * 128); }
        }
        if constexpr (u >= 1 && ks >= 2 && ks < 10) {
          constexpr int i = ks - 2;
          const f32x16& pa = acc[(u - 1) & 1];
          pq[t][u - 1][i] = pk(__builtin_amdgcn_exp2f(__builtin_fmaf(pa[2 * i], c1, nb)), __builtin_amdgcn_exp2f(__builtin_fmaf(pa[2 * i + 1], c1, nb)));
          asm volatile("" : "+v"(pq[t][u - 1][i]));
        }
        SB();
      });
    }
    STAMP(3);
    CSTAMP(15);
    float inv[2];
    {
      for (unsigned it = 0; it < SPIN_MAX; ++it) {
        if (*(LAS volatile unsigned*)(L + L_KSC) == 3u) break;
        __builtin_amdgcn_s_sleep(1);
      }
      asm volatile("" ::: "memory");
      f32x16 d0 = {}, d1 = {};
#pragma unroll
      for (int us = 0; us < 12; ++us) {
        const u32x4 a = ld128(L + L_TAB + h * 16 + us * 32);
        const unsigned* p0 = &pq[0][us >> 1][(us & 1) * 4];
        const unsigned* p1 = &pq[1][us >> 1][(us & 1) * 4];
        d0 = mfma(a, (u32x4){p0[0], p0[1], p0[2], p0[3]}, d0);
        d1 = mfma(a, (u32x4){p1[0], p1[1], p1[2], p1[3]}, d1);
      }
      inv[0] = 1.0f / (d0[0] + 1e-6f); inv[1] = 1.0f / (d1[0] + 1e-6f);
      asm volatile("" : "+v"(inv[0]), "+v"(inv[1]));
    }
    STAMP(7);
    BAR();
    STAMP(4);
    const unsigned sa = w * 16384 + r32 * 128 + ((r32 & 7) << 4) + h * 8;
    {
      f32x16 acc[2][2];
      u32x4 A[6];
      const unsigned kb = L_V + l * 16;
      unsigned fr = 0;
#pragma unroll
      for (int i = 0; i < 6; ++i) A[i] = ld128(L + kb + i * 1024);
      SB();
      sfor<0, 82>([&](auto ic) {
        constexpr int n = decltype(ic)::v, c = n / 12, us = n % 12, pb = c & 1;
        if constexpr (n < 72) {
          const unsigned* p0 = &pq[0][us >> 1][(us & 1) * 4];
          const unsigned* p1 = &pq[1][us >> 1][(us & 1) * 4];
          acc[pb][0] = mfma(A[n % 6], (u32x4){p0[0], p0[1], p0[2], p0[3]}, us == 0 ? (f32x16){} : acc[pb][0]);
          acc[pb][1] = mfma(A[n % 6], (u32x4){p1[0], p1[1], p1[2], p1[3]}, us == 0 ? (f32x16){} : acc[pb][1]);
          if constexpr (n + 6 < 72) A[n % 6] = ld128(L + kb + (n + 6) * 1024);
        }
        if constexpr (n == 54) fr = *(LAS volatile unsigned*)(L + L_FR + w * 4);
        if constexpr (n == 60) {
          while (fr == 0u) fr = *(LAS volatile unsigned*)(L + L_FR + w * 4);
        }
        if constexpr (c >= 1 && us >= 1 && us <= 8) {
          constexpr int dt = c - 1, e = us - 1, t = e >> 2, qq = e & 3;
          const f32x16& a = acc[pb ^ 1][t];
          u32x2 o = {pk(a[4 * qq] * inv[t], a[4 * qq + 1] * inv[t]), pk(a[4 * qq + 2] * inv[t], a[4 * qq + 3] * inv[t])};
          *(LAS u32x2*)(L + (sa ^ ((((dt & 1) * 4 + qq)) << 4)) + ((dt >> 1) & 1) * 8192 + t * 4096) = o;
          if constexpr ((dt & 1) && us == 8) *(LAS volatile unsigned*)(L + L_SF + w * 4) = (dt >> 1) + 1;
        }
        SB();
      });
    }
    STAMP(5);
    __builtin_amdgcn_s_waitcnt(0);
    STAMP(9);
    DUMPTS();
    __builtin_amdgcn_s_waitcnt(0);
  } else {
    const int g = w - 4;
    char* const hello = ws + W_HEL + (size_t)head * 64;
    auto storer = [&]() {
      const __amdgpu_buffer_rsrc_t sO = __builtin_amdgcn_make_buffer_rsrc((void*)(out + hoff + (size_t)i8 * 256 * ROWB), 0, 256 * ROWB, 0x00020000);
      const unsigned sr = g * 16384 + (l >> 3) * 128 + (((l & 7) ^ ((l >> 3) & 7)) << 4);
      const unsigned so = (g * 64 + (l >> 3)) * ROWB + (l & 7) * 16;
#pragma unroll
      for (int cp = 0; cp < 3; ++cp) {
        for (unsigned it = 0; it < SPIN_MAX; ++it) {
          if (*(LAS volatile unsigned*)(L + L_SF + g * 4) > (unsigned)cp) break;
          __builtin_amdgcn_s_sleep(2);
        }
        asm volatile("" ::: "memory");
        u32x4 val[8];
#pragma unroll
        for (int i = 0; i < 8; ++i) val[i] = ld128(L + sr + (cp & 1) * 8192 + i * 1024);
        WAITL();
        if (cp == 0) *(LAS volatile unsigned*)(L + L_FR + g * 4) = 1u;
#pragma unroll
        for (int i = 0; i < 8; ++i) __builtin_amdgcn_raw_buffer_store_b128(val[i], sO, so + i * 8 * ROWB + cp * 128, 0, NT_OUT);
      }
      __builtin_amdgcn_s_waitcnt(0);
      STAMP(9);
    };
    if (w == 7) {
      unsigned vv[3];
#pragma unroll
      for (int p = 0; p < 3; ++p) { int Lc = p * 64 + l, row = Lc / 24, c = Lc % 24; vv[p] = row * ROWB + ((c ^ (((row >> 1) & 1) << 2)) << 4); }
      const __amdgpu_buffer_rsrc_t sV = __builtin_amdgcn_make_buffer_rsrc((void*)(v + hoff + (size_t)sc * 512 * ROWB), 0, 512 * ROWB, 0x00020000);
      auto issueV = [&](int j, int slot) {
#pragma unroll
        for (int a = 0; a < 4; ++a)
#pragma unroll
          for (int p = 0; p < 3; ++p) dma<NT_IN>(lbase + L_V + slot * TILE + a * 3072 + p * 1024, vv[p], sV, j * TILE + a * 3072);
      };
      const u64 old = ald(hello + i8 * 8);
      const u64 n = old * 0x9E3779B97F4A7C15ull + 0x632BE59BD9B4E019ull;
      if (l == 0) {
        ast(hello + i8 * 8, n);
        *(LAS u64*)(L + L_NON) = n; *(LAS unsigned*)(L + L_CNT) = 0u; *(LAS u64*)(L + L_FLG) = 0ull; *(LAS unsigned*)(L + L_KSC) = 0u;
        *(LAS u32x4*)(L + L_SF) = (u32x4){0u, 0u, 0u, 0u}; *(LAS u32x4*)(L + L_FR) = (u32x4){0u, 0u, 0u, 0u};
        *(LAS u32x4*)(L + L_Q0) = (u32x4){0u, 0u, 0u, 0u}; *(LAS unsigned*)(L + L_DRN) = 0u;
      }
      WAITV(0);
#pragma unroll 1
      for (int j = 0; j < 3; ++j) issueV(j, j);
      BAR();
      BAR();
      int islot = 2;
#pragma unroll 1
      for (int r = 0; r <= 16; ++r) {
        if (r >= 1 && r <= 13) issueV(r + 2, islot);
        else if (r >= 14) issue12(IC<NT_IN>{}, lbase + L_V + (r - 13) * TILE, sQ, 2 * (r - 14) * TILE);
        islot = islot == 4 ? 0 : islot + 1;
        RCLOSE(r, WAITV(36));
      }
      issue12(IC<NT_IN>{}, lbase + L_V + 4 * TILE, sQ, 6 * TILE);
      DUMPACC();
      WAITV(36); if (l == 0) *(LAS volatile unsigned*)(L + L_FLG + 4) = 1u;
      WAITV(24); if (l == 0) *(LAS volatile unsigned*)(L + L_FLG + 4) = 2u;
      WAITV(12); if (l == 0) *(LAS volatile unsigned*)(L + L_FLG + 4) = 3u;
      WAITV(0);  if (l == 0) *(LAS volatile unsigned*)(L + L_FLG + 4) = 4u;
      if (DRAINW) {
        for (unsigned it = 0; it < SPIN_MAX; ++it) {
          if (*(LAS volatile unsigned*)(L + L_DRN) >= DRAINW) break;
          __builtin_amdgcn_s_sleep(1);
        }
      }
#pragma unroll 1
      for (int j = 0; j < 4; ++j) {
        for (unsigned it = 0; it < SPIN_MAX; ++it) {
          if (*(LAS volatile unsigned*)(L + L_Q0 + j * 4) != 0u) break;
          __builtin_amdgcn_s_sleep(1);
        }
        asm volatile("" ::: "memory");
        issue12(IC<NT_IN>{}, lbase + L_V + (1 + j) * TILE, sQ, (2 * j + 1) * TILE);
      }
      WAITV(36); if (l == 0) *(LAS volatile unsigned*)(L + L_FLG + 4) = 5u;
      WAITV(24); if (l == 0) *(LAS volatile unsigned*)(L + L_FLG + 4) = 6u;
      WAITV(12); if (l == 0) *(LAS volatile unsigned*)(L + L_FLG + 4) = 7u;
      WAITV(0);  if (l == 0) *(LAS volatile unsigned*)(L + L_FLG + 4) = 8u;
      BAR();
      storer();
    } else {
      BAR();
      BAR();
      const int blk = (l >> 4) & 1, q4 = (l & 15) >> 2, p4 = l & 3;
      unsigned xa[2], va[2];
#pragma unroll
      for (int t = 0; t < 2; ++t) xa[t] = L_X + (8 * h + 4 * t + q4) * 192 + ((((4 * blk + p4) ^ (4 * h + 2 * t + (q4 >> 1)))) << 3);
#pragma unroll
      for (int e = 0; e < 2; ++e) va[e] = L_V + (8 * h + q4) * ROWB + (2 * g + (e ^ ((q4 >> 1) & 1))) * 64 + blk * 32 + p4 * 8;
      f32x16 acc[2][3] = {};
      float ksum = 0.f, ksum2 = 0.f;
      u64 view = 0;
      u32x4 B[2][2], A[2][2][3];
      auto ldB = [&](unsigned vb) {
#pragma unroll
        for (int ks = 0; ks < 2; ++ks)
#pragma unroll
          for (int e = 0; e < 2; ++e) {
            const unsigned ad = va[e] + vb;
            u32x2 b0 = ldtr(L + ad + ks * 6144), b1 = ldtr(L + ad + ks * 6144 + 1536);
            B[ks][e] = (u32x4){b0[0], b0[1], b1[0], b1[1]};
          }
      };
      int vslot = 0;
      auto g2_round = [&](auto par, auto domul, auto doread) {
        constexpr int P = decltype(par)::v;
        constexpr bool M = decltype(domul)::v, R = decltype(doread)::v;
        SB();
        sfor<0, 12>([&](auto ic) {
          constexpr int n = decltype(ic)::v, ks = n / 6, j = (n % 6) >> 1, e = n & 1;
          if constexpr (M) acc[e][j] = mfma(A[P ^ 1][ks][j], B[ks][e], acc[e][j]);
          if constexpr (R && n < 6) {
            u32x2 a0 = ldtr(L + xa[0] + P * 6144 + (n / 3) * 3072 + (n % 3) * 64), a1 = ldtr(L + xa[1] + P * 6144 + (n / 3) * 3072 + (n % 3) * 64);
            A[P][n / 3][n % 3] = (u32x4){a0[0], a0[1], a1[0], a1[1]};
          }
          SB();
        });
        if constexpr (R) { ldB(vslot * TILE); vslot = vslot == 4 ? 0 : vslot + 1; }
        if constexpr (M) {
          auto sum = [&](auto jc) {
            constexpr int j = decltype(jc)::v;
#pragma unroll
            for (int ks = 0; ks < 2; ++ks)
#pragma unroll
              for (int i = 0; i < 4; ++i) {
                if (KSV) { ksum += bflo(A[P ^ 1][ks][j][i]); ksum2 += bfhi(A[P ^ 1][ks][j][i]); }
                else ksum = dot2(A[P ^ 1][ks][j][i], 0x3f803f80u, ksum);
              }
            asm volatile("" : "+v"(ksum), "+v"(ksum2));
          };
          if (g == 0) sum(IC<0>{}); else if (g == 1) sum(IC<1>{}); else sum(IC<2>{});
        }
      };
      const __amdgpu_buffer_rsrc_t sW = __builtin_amdgcn_make_buffer_rsrc((void*)ws, 0, (int)W_INB, 0x00020000);
      const unsigned p1own = (unsigned)W_P1 + (unsigned)((((head * 2 + mh) * 4 + sc) * 3 + g) * 12) * 1024u;
      auto finslot = [&](int pm, int f) {
        return (unsigned)W_FIN + (unsigned)(head * 72 + ((2 * g + f / 6) * 6 + 3 * pm + ((f % 6) >> 1)) * 2 + (f & 1)) * 1024u;
      };
      const unsigned ksown = (unsigned)W_KS + ((head * 8 + i8) * 3 + g) * 128;
      view = ald(hello + (l & 7) * 8);
      char* const inb = ws + W_INB;
      char* const myin = inb + ((size_t)(head * 8 + i8) * 3 + g) * 128;
      char* const tgt = inb + ((size_t)(head * 8 + (l & 7)) * 3 + g) * 128;
      const bool pub = l >= 8 && l < 16;
      if (G1PRIO) __builtin_amdgcn_s_setprio(G1PRIO);
      RCLOSE(0, );
      const u64 nown = *(LAS u64*)(L + L_NON);
      RCLOSE(1, );
      g2_round(IC<0>{}, IC<0>{}, IC<1>{});
      RCLOSE(2, );
#pragma unroll 1
      for (int r = 3; r <= 15; r += 2) {
        if (r <= 9) {
          const u32x4 e4 = {EMPTY, EMPTY, EMPTY, EMPTY};
          const int j0 = (r - 3) / 2 * 3;
#pragma unroll
          for (int i = 0; i < 3; ++i) {
            const unsigned a = r == 9 ? finslot(mh, 3 * sc + i) : p1own + (unsigned)(j0 < 3 * sc ? j0 + i : j0 + i + 3) * 1024u;
            __builtin_amdgcn_raw_buffer_store_b128(e4, sW, l * 16 + a, 0, 16);
          }
          if (r == 3 && l < 32) __builtin_amdgcn_raw_buffer_store_b32(EMPTY, sW, l * 4, ksown, 16);
        }
        if (r == PUBR) {
          WAITV(0);
          if (pub) ast(tgt + i8 * 8, view);
        }
        g2_round(IC<1>{}, IC<1>{}, IC<1>{});
        RCLOSE(r, );
        g2_round(IC<0>{}, IC<1>{}, IC<1>{});
        RCLOSE(r + 1, );
      }
      const char* const pa = pub ? hello + (l & 7) * 8 : myin + (l & 7) * 8;
      u64 x0 = ald(pa);
      g2_round(IC<1>{}, IC<1>{}, IC<1>{});
      g2_round(IC<0>{}, IC<1>{}, IC<0>{});
      DUMPACC();
      if (G1PRIO) __builtin_amdgcn_s_setprio(0);
      STAMP(2);
      WAITL();
      if (l == 0) __hip_atomic_fetch_add((LAS unsigned*)(L + L_CNT), 1u, __ATOMIC_RELAXED, __HIP_MEMORY_SCOPE_WORKGROUP);
      float own[3][8];
#pragma unroll
      for (int f = 0; f < 12; ++f) {
        const f32x16& a = acc[f / 6][(f % 6) >> 1];
        const int o = (f & 1) * 8;
        if (f / 3 != sc) {
          u32x4 val = {ne(pk(a[o], a[o + 1])), ne(pk(a[o + 2], a[o + 3])), ne(pk(a[o + 4], a[o + 5])), ne(pk(a[o + 6], a[o + 7]))};
          __builtin_amdgcn_raw_buffer_store_b128(val, sW, l * 16 + p1own + f * 1024, 0, 16);
        } else {
#pragma unroll
          for (int i = 0; i < 8; ++i) own[f % 3][i] = a[o + i];
        }
      }
      {
        const float ks = halfsum(ksum + ksum2);
        if (l < 32) __builtin_amdgcn_raw_buffer_store_b32(ne(__float_as_uint(ks)), sW, l * 4, ksown, 16);
      }
      WAITV(0);
      if (DRAINW && l == 0) __hip_atomic_fetch_add((LAS unsigned*)(L + L_DRN), 1u, __ATOMIC_RELAXED, __HIP_MEMORY_SCOPE_WORKGROUP);
      STAMP(3);
      for (unsigned it = 0; it < SPIN_MAX; ++it) {
        if (pub && x0 != view) { view = x0; ast(tgt + i8 * 8, view); }
        if (__all(l >= 8 || x0 == nown)) break;
        __builtin_amdgcn_s_sleep(POLL_SLEEP);
        x0 = ald(pa);
      }
      asm volatile("" ::: "memory");
      const u64 xl = ald(hello + (l & 7) * 8);
      float kst;
      for (unsigned it = 0; it < SPIN_MAX; ++it) {
        asm volatile("" ::: "memory");
        unsigned kw[4];
#pragma unroll
        for (int s = 0; s < 4; ++s)
          kw[s] = __builtin_amdgcn_raw_buffer_load_b32(sW, h * 384 + r32 * 4, (unsigned)W_KS + ((head * 8 + 2 * s) * 3 + g) * 128, 16);
        kst = (__uint_as_float(kw[0]) + __uint_as_float(kw[1])) + (__uint_as_float(kw[2]) + __uint_as_float(kw[3]));
        if (__all(max3u(max3u(kw[0], kw[1], kw[2]), kw[3], 0u) != EMPTY)) break;
        __builtin_amdgcn_s_sleep(POLL_SLEEP);
      }
      asm volatile("" ::: "memory");
      if (pub && xl != view) { view = xl; ast(tgt + i8 * 8, view); }
      STAMP(4);
      u32x4 pin[3][3];
      for (unsigned it = 0; it < SPIN_MAX; ++it) {
        asm volatile("" ::: "memory");
#pragma unroll
        for (int s = 0; s < 3; ++s) {
          const int so = s + (s >= sc);
          const unsigned base = (unsigned)W_P1 + (unsigned)(((((head * 2 + mh) * 4 + so) * 3 + g) * 12) + 3 * sc) * 1024u;
#pragma unroll
          for (int i = 0; i < 3; ++i) pin[s][i] = __builtin_amdgcn_raw_buffer_load_b128(sW, l * 16, base + i * 1024, 16);
        }
        unsigned m = 0;
#pragma unroll
        for (int s = 0; s < 3; ++s)
#pragma unroll
          for (int i = 0; i < 3; ++i) m = max3u(max3u(m, pin[s][i][0], pin[s][i][1]), pin[s][i][2], pin[s][i][3]);
        if (__all(m != EMPTY)) break;
      }
      asm volatile("" ::: "memory");
      {
        const int u = 3 * h + g;
        const unsigned off = ((u * 2 + (r32 >> 4)) * 2 + ((r32 >> 2) & 1)) * 16 + (((r32 >> 3) & 1) * 4 + (r32 & 3)) * 2;
        *(LAS unsigned short*)(L + L_TAB + off) = (unsigned short)(pk(kst, 0.f) & 0xffffu);
        if (l == 0) __hip_atomic_fetch_add((LAS unsigned*)(L + L_KSC), 1u, __ATOMIC_RELAXED, __HIP_MEMORY_SCOPE_WORKGROUP);
      }
#pragma unroll
      for (int i = 0; i < 3; ++i) {
#pragma unroll
        for (int s = 0; s < 3; ++s)
#pragma unroll
          for (int e = 0; e < 4; ++e) { own[i][2 * e] += bflo(pin[s][i][e]); own[i][2 * e + 1] += bfhi(pin[s][i][e]); }
        u32x4 val = {ne(pk(own[i][0], own[i][1])), ne(pk(own[i][2], own[i][3])), ne(pk(own[i][4], own[i][5])), ne(pk(own[i][6], own[i][7]))};
        __builtin_amdgcn_raw_buffer_store_b128(val, sW, l * 16 + finslot(mh, 3 * sc + i), 0, 16);
      }
      STAMP(5);
      {
        const unsigned ha = finslot(l & 1, 3 * ((l >> 1) & 3) + 2) + 1020;
        for (unsigned it = 0; it < SPIN_MAX; ++it) {
          asm volatile("" ::: "memory");
          const unsigned x = __builtin_amdgcn_raw_buffer_load_b32(sW, ha, 0, 16);
          if (__all(x != EMPTY)) break;
          __builtin_amdgcn_s_sleep(POLL_SLEEP);
        }
      }
      asm volatile("" ::: "memory");
      STAMP(6);
      u32x4 fin[24];
      const unsigned fb = (unsigned)W_FIN + (unsigned)(head * 72 + g * 24) * 1024u;
      for (unsigned it = 0; it < SPIN_MAX; ++it) {
        asm volatile("" ::: "memory");
#pragma unroll
        for (int i = 0; i < 24; ++i) fin[i] = __builtin_amdgcn_raw_buffer_load_b128(sW, l * 16, fb + i * 1024, 16);
        unsigned m = 0;
#pragma unroll
        for (int i = 0; i < 24; ++i) m = max3u(max3u(m, fin[i][0], fin[i][1]), fin[i][2], fin[i][3]);
        if (__all(m != EMPTY)) break;
      }
      asm volatile("" ::: "memory");
      for (unsigned it = 0; it < SPIN_MAX; ++it) {
        if (__hip_atomic_load((LAS unsigned*)(L + L_CNT), __ATOMIC_RELAXED, __HIP_MEMORY_SCOPE_WORKGROUP) == 7u) break;
        __builtin_amdgcn_s_sleep(1);
      }
      asm volatile("" ::: "memory");
#pragma unroll
      for (int i = 0; i < 24; ++i) *(LAS u32x4*)(L + L_V + (g * 24 + i) * 1024 + l * 16) = fin[i];
      STAMP(7);
      BAR();
      storer();
    }
    DUMPTS();
    __builtin_amdgcn_s_waitcnt(0);
  }
}

extern "C" void kernel_launch(void* const* d_in, const int* in_sizes, int n_in, void* d_out, int out_size, void* d_ws, size_t ws_size,
                              hipStream_t stream) {
  performer_fused<<<256, 512, 0, stream>>>((const char*)d_in[0], (const char*)d_in[1], (const char*)d_in[2], (const char*)d_in[3], (char*)d_out,
                                           (char*)d_ws);
}
```

```cpp
#include <hip/hip_runtime.h>
#include <hip/hip_bf16.h>
#include <stdint.h>

typedef unsigned long long u64;
typedef __attribute__((ext_vector_type(8))) __bf16 bf16x8;
typedef __attribute__((ext_vector_type(2))) __bf16 bf16x2;
typedef __attribute__((ext_vector_type(2))) float f32x2;
typedef __attribute__((ext_vector_type(16))) float f32x16;
typedef __attribute__((ext_vector_type(4))) unsigned u32x4;
typedef __attribute__((ext_vector_type(2))) unsigned u32x2;
typedef __attribute__((ext_vector_type(4))) short s16x4;
#define LAS __attribute__((address_space(3)))
typedef LAS char* lptr;

#define NT_IN 3
#define NT_Q 3
#define NT_OUT 19
#define TS 0

constexpr int ROWB = 384;
constexpr int TILE = 12288;
constexpr int L_K = 0, L_V = 7 * TILE, L_X = L_V + 5 * TILE, L_BIAS = L_X + TILE, L_TAB = L_BIAS + 512, L_CNT = L_TAB + 512, L_NON = L_CNT + 8,
              L_FLG = L_CNT + 16, L_KSC = L_CNT + 32, L_SF = L_CNT + 48, L_FR = L_CNT + 64, L_Q0 = L_CNT + 80, L_DRN = L_CNT + 96, L_END = L_CNT + 112;
constexpr size_t W_P1 = 0;
constexpr size_t W_FIN = 32ull * 2 * 4 * 3 * 12 * 1024;
constexpr size_t W_KS = W_FIN + 32ull * 72 * 1024;
constexpr size_t W_INB = W_KS + 32ull * 8 * 3 * 128;
constexpr size_t W_HEL = W_INB + 256ull * 3 * 128;
constexpr size_t W_TS = W_HEL + 4096;
constexpr unsigned SPIN_MAX = 1u << 22;

__device__ __forceinline__ f32x16 mfma(u32x4 a, u32x4 b, f32x16 c) {
  return __builtin_amdgcn_mfma_f32_32x32x16_bf16(__builtin_bit_cast(bf16x8, a), __builtin_bit_cast(bf16x8, b), c, 0, 0, 0);
}
__device__ __forceinline__ unsigned pk(float a, float b) {
  f32x2 v = {a, b};
  return __builtin_bit_cast(unsigned, __builtin_convertvector(v, bf16x2));
}
__device__ __forceinline__ float dot2(unsigned a, unsigned b, float c) {
  return __builtin_amdgcn_fdot2_f32_bf16(__builtin_bit_cast(bf16x2, a), __builtin_bit_cast(bf16x2, b), c, false);
}
__device__ __forceinline__ u32x4 ld128(lptr p) { return *(LAS u32x4*)p; }
__device__ __forceinline__ u32x2 ldtr(lptr p) { return __builtin_bit_cast(u32x2, __builtin_amdgcn_ds_read_tr16_b64_v4i16((LAS s16x4*)p)); }
__device__ __forceinline__ float bflo(unsigned x) { return __uint_as_float(x << 16); }
__device__ __forceinline__ float bfhi(unsigned x) { return __uint_as_float(x & 0xffff0000u); }
__device__ __forceinline__ float halfsum(float x) {
  auto r = __builtin_amdgcn_permlane32_swap(__float_as_uint(x), __float_as_uint(x), false, false);
  return __uint_as_float(r[0]) + __uint_as_float(r[1]);
}
template <int NT>
__device__ __forceinline__ void dma(unsigned ldsaddr, unsigned voff, __amdgpu_buffer_rsrc_t srd, unsigned soff) {
  if constexpr (NT == 1)
    asm volatile("s_mov_b32 m0, %0\n\ts_nop 0\n\tbuffer_load_dwordx4 %1, %2, %3 offen nt lds" ::"s"(ldsaddr), "v"(voff), "s"(srd), "s"(soff) : "memory");
  else if constexpr (NT == 2)
    asm volatile("s_mov_b32 m0, %0\n\ts_nop 0\n\tbuffer_load_dwordx4 %1, %2, %3 offen sc1 nt lds" ::"s"(ldsaddr), "v"(voff), "s"(srd), "s"(soff) : "memory");
  else if constexpr (NT == 3)
    asm volatile("s_mov_b32 m0, %0\n\ts_nop 0\n\tbuffer_load_dwordx4 %1, %2, %3 offen sc0 sc1 nt lds" ::"s"(ldsaddr), "v"(voff), "s"(srd), "s"(soff) : "memory");
  else
    asm volatile("s_mov_b32 m0, %0\n\ts_nop 0\n\tbuffer_load_dwordx4 %1, %2, %3 offen lds" ::"s"(ldsaddr), "v"(voff), "s"(srd), "s"(soff) : "memory");
}
template <int V> struct IC { static constexpr int v = V; };
template <int I, int N, class F> __device__ __forceinline__ void sfor(F&& f) { if constexpr (I < N) { f(IC<I>{}); sfor<I + 1, N>(f); } }
#define SB() __builtin_amdgcn_sched_barrier(0)
#define G1PRIO 1
#define Q1AUX 0
#define DRAINW 3u
#define G1P 0
#define NS 8
#define KSV 1
#define QR0 3
#define QRN 6
#define POLL_SLEEP 2
#define PUBR 13
constexpr unsigned EMPTY = 0xffffffffu;
__device__ __forceinline__ unsigned ne(unsigned x) { return x < EMPTY - 1u ? x : EMPTY - 1u; }
__device__ __forceinline__ unsigned max3u(unsigned a, unsigned b, unsigned c) { const unsigned m = a > b ? a : b; return m > c ? m : c; }
#define WAITV(n) asm volatile("s_waitcnt vmcnt(" #n ")" ::: "memory")
#define WAITL() asm volatile("s_waitcnt lgkmcnt(0)" ::: "memory")
#define BAR() do { asm volatile("s_waitcnt lgkmcnt(0)" ::: "memory"); __builtin_amdgcn_s_barrier(); asm volatile("" ::: "memory"); } while (0)
typedef __attribute__((address_space(1))) u64 gu64;
__device__ __forceinline__ u64 ald(const char* p) { return __hip_atomic_load((gu64*)p, __ATOMIC_RELAXED, __HIP_MEMORY_SCOPE_AGENT); }
__device__ __forceinline__ void ast(char* p, u64 v) { __hip_atomic_store((gu64*)p, v, __ATOMIC_RELAXED, __HIP_MEMORY_SCOPE_AGENT); }
#define STAMP(i)
#define CSTAMP(i)
#define DUMPTS()
#define TNOW() 0ull

__device__ __forceinline__ void feat(const f32x16& acc, float c1, float nb, unsigned* o) {
#pragma unroll
  for (int i = 0; i < 8; ++i)
    o[i] = pk(__builtin_amdgcn_exp2f(__builtin_fmaf(acc[2 * i], c1, nb)), __builtin_amdgcn_exp2f(__builtin_fmaf(acc[2 * i + 1], c1, nb)));
}
__device__ __forceinline__ float rownorm(const u32x4* f) {
  float s0 = 0.f, s1 = 0.f;
#pragma unroll
  for (int i = 0; i < 12; ++i) {
    s0 = dot2(f[i][0], f[i][0], s0); s1 = dot2(f[i][1], f[i][1], s1);
    s0 = dot2(f[i][2], f[i][2], s0); s1 = dot2(f[i][3], f[i][3], s1);
  }
  return halfsum(s0 + s1);
}

__global__ __launch_bounds__(512, 2) void performer_fused(const char* __restrict__ q, const char* __restrict__ k, const char* __restrict__ v,
                                                          const char* __restrict__ proj, char* __restrict__ out, char* __restrict__ ws) {
  __shared__ __attribute__((aligned(16))) char lds_raw[L_END + (TS ? 1024 : 0)];
  lptr L = (lptr)lds_raw;
  const unsigned lbase = (unsigned)(uintptr_t)L;
  const int b = blockIdx.x;
  const int l = threadIdx.x & 63;
  const int w = __builtin_amdgcn_readfirstlane(threadIdx.x >> 6);
  const int head = (b & 7) * 4 + (b >> 6), i8 = (b >> 3) & 7, mh = i8 & 1, sc = i8 >> 1;
  const int h = l >> 5, r32 = l & 31;
  const float scale = 0.26864248f;
  const float LOG2E = 1.44269504089f;
  const float c1 = scale * LOG2E, cn = -0.5f * scale * scale * LOG2E, c0 = -3.79248125036f;
  const size_t hoff = (size_t)head * 2048 * ROWB;
  STAMP(0);

  unsigned fa[4];
#pragma unroll
  for (int kk = 0; kk < 4; ++kk) fa[kk] = r32 * ROWB + ((((2 * kk + h) ^ ((r32 >> 1) & 7))) << 4);
  auto ldfrags = [&](u32x4* f, unsigned base) {
#pragma unroll
    for (int ks = 0; ks < 12; ++ks) f[ks] = ld128(L + base + fa[ks & 3] + (ks >> 2) * 128);
  };

  unsigned vo[3];
#pragma unroll
  for (int p = 0; p < 3; ++p) { int Lc = p * 64 + l, row = Lc / 24, c = Lc % 24; vo[p] = row * ROWB + ((c ^ (row >> 1)) << 4); }
  auto issue12 = [&](auto nt, unsigned dst, __amdgpu_buffer_rsrc_t srd, unsigned soff) {
#pragma unroll
    for (int a = 0; a < 4; ++a)
#pragma unroll
      for (int p = 0; p < 3; ++p) dma<decltype(nt)::v>(dst + a * 3072 + p * 1024, vo[p] ^ ((a & 1) << 6), srd, soff + a * 3072);
  };
  const __amdgpu_buffer_rsrc_t sQ = __builtin_amdgcn_make_buffer_rsrc((void*)(q + hoff + (size_t)i8 * 256 * ROWB), 0, 256 * ROWB, 0x00020000);
  u64 tprev = 0, acc_c = 0, acc_v = 0, acc_b = 0;
#define RCLOSE(r, waits) do { WAITL(); const u64 ta = TNOW(); waits; const u64 tb = TNOW(); BAR(); const u64 tc = TNOW(); \
    if (TS >= 2 && (r) >= 2 && (r) <= 11) { acc_c += ta - tprev; acc_v += tb - ta; acc_b += tc - tb; } tprev = tc; } while (0)
#define DUMPACC()

  if (w < 4) {
    const __amdgpu_buffer_rsrc_t sK = __builtin_amdgcn_make_buffer_rsrc((void*)(k + hoff + (size_t)sc * 512 * ROWB), 0, 512 * ROWB, 0x00020000);
    const __amdgpu_buffer_rsrc_t sP = __builtin_amdgcn_make_buffer_rsrc((void*)proj, 0, 192 * ROWB, 0x00020000);
    {
      const unsigned par = (w & 1) << 6;
#pragma unroll
      for (int t = 0; t < 3; ++t)
#pragma unroll
        for (int p = 0; p < 3; ++p) dma<0>(lbase + L_K + (3 + t) * TILE + w * 3072 + p * 1024, vo[p] ^ par, sP, (3 * mh + t) * TILE + w * 3072);
#pragma unroll
      for (int t = 0; t < 3; ++t)
#pragma unroll
        for (int p = 0; p < 3; ++p) dma<NT_IN>(lbase + L_K + t * TILE + w * 3072 + p * 1024, vo[p] ^ par, sK, t * TILE + w * 3072);
    }
    WAITV(6);
    BAR();
    u32x4 pf[12], kf[12];
    if (w < 3) {
      ldfrags(pf, L_K + (3 + w) * TILE);
      ldfrags(kf, L_K);
      WAITV(0);
      BAR();
      STAMP(1);
      unsigned xw[4];
#pragma unroll
      for (int qq = 0; qq < 4; ++qq) xw[qq] = L_X + r32 * 192 + w * 64 + ((((2 * qq + h) ^ ((r32 >> 1) & 7))) << 3);
      if (G1P) __builtin_amdgcn_s_setprio(G1P);
      int nslot = 1;
      f32x16 acc[2];
      float nb = 0.f, nbn = 0.f;
      auto g1_round = [&](auto par, auto first, auto last, int r) {
        constexpr int P = decltype(par)::v;
        constexpr bool F = decltype(first)::v, Z = decltype(last)::v;
        const unsigned kb = L_K + nslot * TILE;
        constexpr unsigned xb = (P ^ 1) * 6144;
        nslot = nslot == 6 ? 0 : nslot + 1;
        unsigned o[8];
        auto step = [&](auto ic) {
          constexpr int i = decltype(ic)::v;
          const f32x16& pa = acc[P ^ 1];
          o[i] = pk(__builtin_amdgcn_exp2f(__builtin_fmaf(pa[2 * i], c1, nb)), __builtin_amdgcn_exp2f(__builtin_fmaf(pa[2 * i + 1], c1, nb)));
          if constexpr (i & 1) { u32x2 t = {o[i - 1], o[i]}; *(LAS u32x2*)(L + xb + xw[i >> 1]) = t; }
        };
        SB();
        if constexpr (!F) sfor<0, Z ? 8 : NS>([&](auto ic) { step(ic); SB(); });
        if constexpr (!Z) {
          nbn = *(LAS float*)(L + L_BIAS + (r & 3) * 128 + r32 * 4);
          sfor<0, 12>([&](auto ic) {
            constexpr int ks = decltype(ic)::v;
            acc[P] = mfma(pf[ks], kf[ks], ks == 0 ? (f32x16){} : acc[P]);
            kf[ks] = ld128(L + kb + fa[ks & 3] + (ks >> 2) * 128);
            if constexpr (!F && (ks & 1) == 0 && NS + ks / 2 < 8) step(IC<NS + ks / 2>{});
            SB();
          });
          nb = nbn;
        }
      };
#define G1CLOSE() do { asm volatile("s_waitcnt lgkmcnt(%0)" ::"n"(NS >= 8 ? 13 : 11 - 2 * (7 - NS)) : "memory"); __builtin_amdgcn_s_barrier(); asm volatile("" ::: "memory"); } while (0)
      g1_round(IC<0>{}, IC<1>{}, IC<0>{}, 0);
      G1CLOSE();
#pragma unroll 1
      for (int r = 1; r < 15; r += 2) {
        if (TS && r == 5) STAMP(10);
        if (TS && r == 13) STAMP(11);
        g1_round(IC<1>{}, IC<0>{}, IC<0>{}, r);
        G1CLOSE();
        g1_round(IC<0>{}, IC<0>{}, IC<0>{}, r + 1);
        G1CLOSE();
      }
      g1_round(IC<1>{}, IC<0>{}, IC<0>{}, 15);
      G1CLOSE();
      g1_round(IC<0>{}, IC<0>{}, IC<1>{}, 16);
      BAR();
      if (G1P) __builtin_amdgcn_s_setprio(0);
    } else {
      float mk[16];
      {
        const int dg = (h == ((r32 >> 2) & 1)) ? 4 * (r32 >> 3) + (r32 & 3) : -1;
#pragma unroll
        for (int i = 0; i < 16; ++i) mk[i] = i == dg ? 1.f : 0.f;
      }
      auto putbias = [&](const f32x16& a, int slot) {
        float s0 = 0.f, s1 = 0.f;
#pragma unroll
        for (int i = 0; i < 8; ++i) { s0 = __builtin_fmaf(a[2 * i], mk[2 * i], s0); s1 = __builtin_fmaf(a[2 * i + 1], mk[2 * i + 1], s1); }
        const float n = halfsum(s0 + s1);
        if (l < 32) *(LAS float*)(L + L_BIAS + slot * 128 + l * 4) = __builtin_fmaf(n, cn, c0);
      };
      ldfrags(kf, L_K);
      {
        f32x16 a = {};
#pragma unroll
        for (int ks = 0; ks < 12; ++ks) a = mfma(kf[ks], kf[ks], a);
        putbias(a, 0);
      }
      WAITV(0);
      BAR();
      int islot = 6, nslot = 2;
#pragma unroll 1
      for (int j = 3; j < 6; ++j) issue12(IC<NT_IN>{}, lbase + L_K + j * TILE, sK, j * TILE);
      ldfrags(kf, L_K + TILE);
      u32x4 kg[12];
      auto w3_round = [&](auto nt, const u32x4* cur, u32x4* nxt, __amdgpu_buffer_rsrc_t srd, unsigned soff, int r) {
        const unsigned kb = L_K + nslot * TILE;
        nslot = nslot == 6 ? 0 : nslot + 1;
        const unsigned dst = lbase + L_K + islot * TILE;
        islot = islot == 6 ? 0 : islot + 1;
        f32x16 a;
        sfor<0, 12>([&](auto ic) {
          constexpr int n = decltype(ic)::v, aa = n / 3, p = n % 3;
          a = mfma(cur[n], cur[n], n == 0 ? (f32x16){} : a);
          nxt[n] = ld128(L + kb + fa[n & 3] + (n >> 2) * 128);
          dma<decltype(nt)::v>(dst + aa * 3072 + p * 1024, vo[p] ^ ((aa & 1) << 6), srd, soff + aa * 3072);
          SB();
        });
        putbias(a, (r + 1) & 3);
        RCLOSE(r, if (r <= 12) WAITV(36));
      };
#pragma unroll 1
      for (int r = 0; r < 10; r += 2) {
        w3_round(IC<NT_IN>{}, kf, kg, sK, (r + 6) * TILE, r);
        w3_round(IC<NT_IN>{}, kg, kf, sK, (r + 7) * TILE, r + 1);
      }
#pragma unroll 1
      for (int r = 10; r < 16; r += 2) {
        w3_round(IC<0>{}, kf, kg, sP, (r - 10) * TILE, r);
        w3_round(IC<0>{}, kg, kf, sP, (r - 9) * TILE, r + 1);
      }
      RCLOSE(16, );
      WAITV(0);
      if (l == 0) *(LAS volatile unsigned*)(L + L_FLG) = 1u;
    }
    DUMPACC();
    for (unsigned it = 0; it < SPIN_MAX; ++it) {
      const unsigned f0 = *(LAS volatile unsigned*)(L + L_FLG), f1 = *(LAS volatile unsigned*)(L + L_FLG + 4);
      if (f0 != 0u && f1 > (unsigned)w) break;
      __builtin_amdgcn_s_sleep(1);
    }
    asm volatile("" ::: "memory");
    STAMP(2);
    CSTAMP(14);
    unsigned pq[2][6][8];
    const unsigned qslot = L_V + (1 + w) * TILE;
#pragma unroll
    for (int t = 0; t < 2; ++t) {
      if (t == 1) {
        for (unsigned it = 0; it < SPIN_MAX; ++it) {
          if (*(LAS volatile unsigned*)(L + L_FLG + 4) > 4u + (unsigned)w) break;
          __builtin_amdgcn_s_sleep(1);
        }
        asm volatile("" ::: "memory");
      }
      ldfrags(kf, qslot);
      const u32x4* qf = kf;
      u32x4 A[6];
#pragma unroll
      for (int i = 0; i < 6; ++i) A[i] = ld128(L + L_K + 2 * TILE + fa[i & 3] + (i >> 2) * 128);
      WAITL();
      if (t == 0) *(LAS volatile unsigned*)(L + L_Q0 + w * 4) = 1u;
      else if (l == 0) __hip_atomic_fetch_add((LAS unsigned*)(L + L_CNT), 1u, __ATOMIC_RELAXED, __HIP_MEMORY_SCOPE_WORKGROUP);
      STAMP(6 + 2 * t);
      const float nb = __builtin_fmaf(rownorm(qf), cn, c0);
      f32x16 acc[2];
      SB();
      sfor<0, 84>([&](auto ic) {
        constexpr int n = decltype(ic)::v, u = n / 12, ks = n % 12;
        if constexpr (n < 72) {
          acc[u & 1] = mfma(A[n % 6], qf[ks], ks == 0 ? (f32x16){} : acc[u & 1]);
          if constexpr (n + 6 < 72) { constexpr int m = n + 6, uu = m / 12, kk = m % 12; A[n % 6] = ld128(L + L_K + ((2 + uu) % 7) * TILE + fa[kk & 3] + (kk >> 2) * 128); }
        }
        if constexpr (u >= 1 && ks >= 2 && ks < 10) {
          constexpr int i = ks - 2;
          const f32x16& pa = acc[(u - 1) & 1];
          pq[t][u - 1][i] = pk(__builtin_amdgcn_exp2f(__builtin_fmaf(pa[2 * i], c1, nb)), __builtin_amdgcn_exp2f(__builtin_fmaf(pa[2 * i + 1], c1, nb)));
          asm volatile("" : "+v"(pq[t][u - 1][i]));
        }
        SB();
      });
    }
    STAMP(3);
    CSTAMP(15);
    float inv[2];
    {
      for (unsigned it = 0; it < SPIN_MAX; ++it) {
        if (*(LAS volatile unsigned*)(L + L_KSC) == 3u) break;
        __builtin_amdgcn_s_sleep(1);
      }
      asm volatile("" ::: "memory");
      f32x16 d0 = {}, d1 = {};
#pragma unroll
      for (int us = 0; us < 12; ++us) {
        const u32x4 a = ld128(L + L_TAB + h * 16 + us * 32);
        const unsigned* p0 = &pq[0][us >> 1][(us & 1) * 4];
        const unsigned* p1 = &pq[1][us >> 1][(us & 1) * 4];
        d0 = mfma(a, (u32x4){p0[0], p0[1], p0[2], p0[3]}, d0);
        d1 = mfma(a, (u32x4){p1[0], p1[1], p1[2], p1[3]}, d1);
      }
      inv[0] = 1.0f / (d0[0] + 1e-6f); inv[1] = 1.0f / (d1[0] + 1e-6f);
      asm volatile("" : "+v"(inv[0]), "+v"(inv[1]));
    }
    STAMP(7);
    BAR();
    STAMP(4);
    const unsigned sa = w * 16384 + r32 * 128 + ((r32 & 7) << 4) + h * 8;
    {
      f32x16 acc[2][2];
      u32x4 A[6];
      const unsigned kb = L_V + l * 16;
      unsigned fr = 0;
#pragma unroll
      for (int i = 0; i < 6; ++i) A[i] = ld128(L + kb + i * 1024);
      SB();
      sfor<0, 82>([&](auto ic) {
        constexpr int n = decltype(ic)::v, c = n / 12, us = n % 12, pb = c & 1;
        if constexpr (n < 72) {
          const unsigned* p0 = &pq[0][us >> 1][(us & 1) * 4];
          const unsigned* p1 = &pq[1][us >> 1][(us & 1) * 4];
          acc[pb][0] = mfma(A[n % 6], (u32x4){p0[0], p0[1], p0[2], p0[3]}, us == 0 ? (f32x16){} : acc[pb][0]);
          acc[pb][1] = mfma(A[n % 6], (u32x4){p1[0], p1[1], p1[2], p1[3]}, us == 0 ? (f32x16){} : acc[pb][1]);
          if constexpr (n + 6 < 72) A[n % 6] = ld128(L + kb + (n + 6) * 1024);
        }
        if constexpr (n == 54) fr = *(LAS volatile unsigned*)(L + L_FR + w * 4);
        if constexpr (n == 60) {
          while (fr == 0u) fr = *(LAS volatile unsigned*)(L + L_FR + w * 4);
        }
        if constexpr (c >= 1 && us >= 1 && us <= 8) {
          constexpr int dt = c - 1, e = us - 1, t = e >> 2, qq = e & 3;
          const f32x16& a = acc[pb ^ 1][t];
          u32x2 o = {pk(a[4 * qq] * inv[t], a[4 * qq + 1] * inv[t]), pk(a[4 * qq + 2] * inv[t], a[4 * qq + 3] * inv[t])};
          *(LAS u32x2*)(L + (sa ^ ((((dt & 1) * 4 + qq)) << 4)) + ((dt >> 1) & 1) * 8192 + t * 4096) = o;
          if constexpr ((dt & 1) && us == 8) *(LAS volatile unsigned*)(L + L_SF + w * 4) = (dt >> 1) + 1;
        }
        SB();
      });
    }
    STAMP(5);
    __builtin_amdgcn_s_waitcnt(0);
    STAMP(9);
    DUMPTS();
    __builtin_amdgcn_s_waitcnt(0);
  } else {
    const int g = w - 4;
    char* const hello = ws + W_HEL + (size_t)head * 64;
    auto storer = [&]() {
      const __amdgpu_buffer_rsrc_t sO = __builtin_amdgcn_make_buffer_rsrc((void*)(out + hoff + (size_t)i8 * 256 * ROWB), 0, 256 * ROWB, 0x00020000);
      const unsigned sr = g * 16384 + (l >> 3) * 128 + (((l & 7) ^ ((l >> 3) & 7)) << 4);
      const unsigned so = (g * 64 + (l >> 3)) * ROWB + (l & 7) * 16;
#pragma unroll
      for (int cp = 0; cp < 3; ++cp) {
        for (unsigned it = 0; it < SPIN_MAX; ++it) {
          if (*(LAS volatile unsigned*)(L + L_SF + g * 4) > (unsigned)cp) break;
          __builtin_amdgcn_s_sleep(2);
        }
        asm volatile("" ::: "memory");
        u32x4 val[8];
#pragma unroll
        for (int i = 0; i < 8; ++i) val[i] = ld128(L + sr + (cp & 1) * 8192 + i * 1024);
        WAITL();
        if (cp == 0) *(LAS volatile unsigned*)(L + L_FR + g * 4) = 1u;
#pragma unroll
        for (int i = 0; i < 8; ++i) __builtin_amdgcn_raw_buffer_store_b128(val[i], sO, so + i * 8 * ROWB + cp * 128, 0, NT_OUT);
      }
      __builtin_amdgcn_s_waitcnt(0);
      STAMP(9);
    };
    if (w == 7) {
      unsigned vv[3];
#pragma unroll
      for (int p = 0; p < 3; ++p) { int Lc = p * 64 + l, row = Lc / 24, c = Lc % 24; vv[p] = row * ROWB + ((c ^ (((row >> 1) & 1) << 2)) << 4); }
      const __amdgpu_buffer_rsrc_t sV = __builtin_amdgcn_make_buffer_rsrc((void*)(v + hoff + (size_t)sc * 512 * ROWB), 0, 512 * ROWB, 0x00020000);
      auto issueV = [&](int j, int slot) {
#pragma unroll
        for (int a = 0; a < 4; ++a)
#pragma unroll
          for (int p = 0; p < 3; ++p) dma<NT_IN>(lbase + L_V + slot * TILE + a * 3072 + p * 1024, vv[p], sV, j * TILE + a * 3072);
      };
      const u64 old = ald(hello + i8 * 8);
      const u64 n = old * 0x9E3779B97F4A7C15ull + 0x632BE59BD9B4E019ull;
      if (l == 0) {
        ast(hello + i8 * 8, n);
        *(LAS u64*)(L + L_NON) = n; *(LAS unsigned*)(L + L_CNT) = 0u; *(LAS u64*)(L + L_FLG) = 0ull; *(LAS unsigned*)(L + L_KSC) = 0u;
        *(LAS u32x4*)(L + L_SF) = (u32x4){0u, 0u, 0u, 0u}; *(LAS u32x4*)(L + L_FR) = (u32x4){0u, 0u, 0u, 0u};
        *(LAS u32x4*)(L + L_Q0) = (u32x4){0u, 0u, 0u, 0u}; *(LAS unsigned*)(L + L_DRN) = 0u;
      }
      WAITV(0);
#pragma unroll 1
      for (int j = 0; j < 3; ++j) issueV(j, j);
      BAR();
      BAR();
      int islot = 2;
#pragma unroll 1
      for (int r = 0; r <= 16; ++r) {
        if (r >= 1 && r <= 13) issueV(r + 2, islot);
        else if (r >= 14) issue12(IC<NT_Q>{}, lbase + L_V + (r - 13) * TILE, sQ, 2 * (r - 14) * TILE);
        islot = islot == 4 ? 0 : islot + 1;
        RCLOSE(r, WAITV(36));
      }
      issue12(IC<NT_Q>{}, lbase + L_V + 4 * TILE, sQ, 6 * TILE);
      DUMPACC();
      WAITV(36); if (l == 0) *(LAS volatile unsigned*)(L + L_FLG + 4) = 1u;
      WAITV(24); if (l == 0) *(LAS volatile unsigned*)(L + L_FLG + 4) = 2u;
      WAITV(12); if (l == 0) *(LAS volatile unsigned*)(L + L_FLG + 4) = 3u;
      WAITV(0);  if (l == 0) *(LAS volatile unsigned*)(L + L_FLG + 4) = 4u;
      if (DRAINW) {
        for (unsigned it = 0; it < SPIN_MAX; ++it) {
          if (*(LAS volatile unsigned*)(L + L_DRN) >= DRAINW) break;
          __builtin_amdgcn_s_sleep(1);
        }
      }
#pragma unroll 1
      for (int j = 0; j < 4; ++j) {
        for (unsigned it = 0; it < SPIN_MAX; ++it) {
          if (*(LAS volatile unsigned*)(L + L_Q0 + j * 4) != 0u) break;
          __builtin_amdgcn_s_sleep(1);
        }
        asm volatile("" ::: "memory");
        issue12(IC<NT_Q>{}, lbase + L_V + (1 + j) * TILE, sQ, (2 * j + 1) * TILE);
      }
      WAITV(36); if (l == 0) *(LAS volatile unsigned*)(L + L_FLG + 4) = 5u;
      WAITV(24); if (l == 0) *(LAS volatile unsigned*)(L + L_FLG + 4) = 6u;
      WAITV(12); if (l == 0) *(LAS volatile unsigned*)(L + L_FLG + 4) = 7u;
      WAITV(0);  if (l == 0) *(LAS volatile unsigned*)(L + L_FLG + 4) = 8u;
      BAR();
      storer();
    } else {
      BAR();
      BAR();
      const int blk = (l >> 4) & 1, q4 = (l & 15) >> 2, p4 = l & 3;
      unsigned xa[2], va[2];
#pragma unroll
      for (int t = 0; t < 2; ++t) xa[t] = L_X + (8 * h + 4 * t + q4) * 192 + ((((4 * blk + p4) ^ (4 * h + 2 * t + (q4 >> 1)))) << 3);
#pragma unroll
      for (int e = 0; e < 2; ++e) va[e] = L_V + (8 * h + q4) * ROWB + (2 * g + (e ^ ((q4 >> 1) & 1))) * 64 + blk * 32 + p4 * 8;
      f32x16 acc[2][3] = {};
      float ksum = 0.f, ksum2 = 0.f;
      u64 view = 0;
      u32x4 B[2][2], A[2][2][3];
      auto ldB = [&](unsigned vb) {
#pragma unroll
        for (int ks = 0; ks < 2; ++ks)
#pragma unroll
          for (int e = 0; e < 2; ++e) {
            const unsigned ad = va[e] + vb;
            u32x2 b0 = ldtr(L + ad + ks * 6144), b1 = ldtr(L + ad + ks * 6144 + 1536);
            B[ks][e] = (u32x4){b0[0], b0[1], b1[0], b1[1]};
          }
      };
      int vslot = 0;
      auto g2_round = [&](auto par, auto domul, auto doread) {
        constexpr int P = decltype(par)::v;
        constexpr bool M = decltype(domul)::v, R = decltype(doread)::v;
        SB();
        sfor<0, 12>([&](auto ic) {
          constexpr int n = decltype(ic)::v, ks = n / 6, j = (n % 6) >> 1, e = n & 1;
          if constexpr (M) acc[e][j] = mfma(A[P ^ 1][ks][j], B[ks][e], acc[e][j]);
          if constexpr (R && n < 6) {
            u32x2 a0 = ldtr(L + xa[0] + P * 6144 + (n / 3) * 3072 + (n % 3) * 64), a1 = ldtr(L + xa[1] + P * 6144 + (n / 3) * 3072 + (n % 3) * 64);
            A[P][n / 3][n % 3] = (u32x4){a0[0], a0[1], a1[0], a1[1]};
          }
          SB();
        });
        if constexpr (R) { ldB(vslot * TILE); vslot = vslot == 4 ? 0 : vslot + 1; }
        if constexpr (M) {
          auto sum = [&](auto jc) {
            constexpr int j = decltype(jc)::v;
#pragma unroll
            for (int ks = 0; ks < 2; ++ks)
#pragma unroll
              for (int i = 0; i < 4; ++i) {
                if (KSV) { ksum += bflo(A[P ^ 1][ks][j][i]); ksum2 += bfhi(A[P ^ 1][ks][j][i]); }
                else ksum = dot2(A[P ^ 1][ks][j][i], 0x3f803f80u, ksum);
              }
            asm volatile("" : "+v"(ksum), "+v"(ksum2));
          };
          if (g == 0) sum(IC<0>{}); else if (g == 1) sum(IC<1>{}); else sum(IC<2>{});
        }
      };
      const __amdgpu_buffer_rsrc_t sW = __builtin_amdgcn_make_buffer_rsrc((void*)ws, 0, (int)W_INB, 0x00020000);
      const unsigned p1own = (unsigned)W_P1 + (unsigned)((((head * 2 + mh) * 4 + sc) * 3 + g) * 12) * 1024u;
      auto finslot = [&](int pm, int f) {
        return (unsigned)W_FIN + (unsigned)(head * 72 + ((2 * g + f / 6) * 6 + 3 * pm + ((f % 6) >> 1)) * 2 + (f & 1)) * 1024u;
      };
      const unsigned ksown = (unsigned)W_KS + ((head * 8 + i8) * 3 + g) * 128;
      view = ald(hello + (l & 7) * 8);
      char* const inb = ws + W_INB;
      char* const myin = inb + ((size_t)(head * 8 + i8) * 3 + g) * 128;
      char* const tgt = inb + ((size_t)(head * 8 + (l & 7)) * 3 + g) * 128;
      const bool pub = l >= 8 && l < 16;
      if (G1PRIO) __builtin_amdgcn_s_setprio(G1PRIO);
      RCLOSE(0, );
      const u64 nown = *(LAS u64*)(L + L_NON);
      RCLOSE(1, );
      g2_round(IC<0>{}, IC<0>{}, IC<1>{});
      RCLOSE(2, );
#pragma unroll 1
      for (int r = 3; r <= 15; r += 2) {
        if (r <= 9) {
          const u32x4 e4 = {EMPTY, EMPTY, EMPTY, EMPTY};
          const int j0 = (r - 3) / 2 * 3;
#pragma unroll
          for (int i = 0; i < 3; ++i) {
            const unsigned a = r == 9 ? finslot(mh, 3 * sc + i) : p1own + (unsigned)(j0 < 3 * sc ? j0 + i : j0 + i + 3) * 1024u;
            __builtin_amdgcn_raw_buffer_store_b128(e4, sW, l * 16 + a, 0, 16);
          }
          if (r == 3 && l < 32) __builtin_amdgcn_raw_buffer_store_b32(EMPTY, sW, l * 4, ksown, 16);
        }
        if (r == PUBR) {
          WAITV(0);
          if (pub) ast(tgt + i8 * 8, view);
        }
        g2_round(IC<1>{}, IC<1>{}, IC<1>{});
        RCLOSE(r, );
        g2_round(IC<0>{}, IC<1>{}, IC<1>{});
        RCLOSE(r + 1, );
      }
      const char* const pa = pub ? hello + (l & 7) * 8 : myin + (l & 7) * 8;
      u64 x0 = ald(pa);
      g2_round(IC<1>{}, IC<1>{}, IC<1>{});
      g2_round(IC<0>{}, IC<1>{}, IC<0>{});
      DUMPACC();
      if (G1PRIO) __builtin_amdgcn_s_setprio(0);
      STAMP(2);
      WAITL();
      if (l == 0) __hip_atomic_fetch_add((LAS unsigned*)(L + L_CNT), 1u, __ATOMIC_RELAXED, __HIP_MEMORY_SCOPE_WORKGROUP);
      float own[3][8];
#pragma unroll
      for (int f = 0; f < 12; ++f) {
        const f32x16& a = acc[f / 6][(f % 6) >> 1];
        const int o = (f & 1) * 8;
        if (f / 3 != sc) {
          u32x4 val = {ne(pk(a[o], a[o + 1])), ne(pk(a[o + 2], a[o + 3])), ne(pk(a[o + 4], a[o + 5])), ne(pk(a[o + 6], a[o + 7]))};
          __builtin_amdgcn_raw_buffer_store_b128(val, sW, l * 16 + p1own + f * 1024, 0, 16);
        } else {
#pragma unroll
          for (int i = 0; i < 8; ++i) own[f % 3][i] = a[o + i];
        }
      }
      {
        const float ks = halfsum(ksum + ksum2);
        if (l < 32) __builtin_amdgcn_raw_buffer_store_b32(ne(__float_as_uint(ks)), sW, l * 4, ksown, 16);
      }
      WAITV(0);
      if (DRAINW && l == 0) __hip_atomic_fetch_add((LAS unsigned*)(L + L_DRN), 1u, __ATOMIC_RELAXED, __HIP_MEMORY_SCOPE_WORKGROUP);
      STAMP(3);
      for (unsigned it = 0; it < SPIN_MAX; ++it) {
        if (pub && x0 != view) { view = x0; ast(tgt + i8 * 8, view); }
        if (__all(l >= 8 || x0 == nown)) break;
        __builtin_amdgcn_s_sleep(POLL_SLEEP);
        x0 = ald(pa);
      }
      asm volatile("" ::: "memory");
      const u64 xl = ald(hello + (l & 7) * 8);
      float kst;
      for (unsigned it = 0; it < SPIN_MAX; ++it) {
        asm volatile("" ::: "memory");
        unsigned kw[4];
#pragma unroll
        for (int s = 0; s < 4; ++s)
          kw[s] = __builtin_amdgcn_raw_buffer_load_b32(sW, h * 384 + r32 * 4, (unsigned)W_KS + ((head * 8 + 2 * s) * 3 + g) * 128, 16);
        kst = (__uint_as_float(kw[0]) + __uint_as_float(kw[1])) + (__uint_as_float(kw[2]) + __uint_as_float(kw[3]));
        if (__all(max3u(max3u(kw[0], kw[1], kw[2]), kw[3], 0u) != EMPTY)) break;
        __builtin_amdgcn_s_sleep(POLL_SLEEP);
      }
      asm volatile("" ::: "memory");
      if (pub && xl != view) { view = xl; ast(tgt + i8 * 8, view); }
      STAMP(4);
      u32x4 pin[3][3];
      for (unsigned it = 0; it < SPIN_MAX; ++it) {
        asm volatile("" ::: "memory");
#pragma unroll
        for (int s = 0; s < 3; ++s) {
          const int so = s + (s >= sc);
          const unsigned base = (unsigned)W_P1 + (unsigned)(((((head * 2 + mh) * 4 + so) * 3 + g) * 12) + 3 * sc) * 1024u;
#pragma unroll
          for (int i = 0; i < 3; ++i) pin[s][i] = __builtin_amdgcn_raw_buffer_load_b128(sW, l * 16, base + i * 1024, 16);
        }
        unsigned m = 0;
#pragma unroll
        for (int s = 0; s < 3; ++s)
#pragma unroll
          for (int i = 0; i < 3; ++i) m = max3u(max3u(m, pin[s][i][0], pin[s][i][1]), pin[s][i][2], pin[s][i][3]);
        if (__all(m != EMPTY)) break;
      }
      asm volatile("" ::: "memory");
      {
        const int u = 3 * h + g;
        const unsigned off = ((u * 2 + (r32 >> 4)) * 2 + ((r32 >> 2) & 1)) * 16 + (((r32 >> 3) & 1) * 4 + (r32 & 3)) * 2;
        *(LAS unsigned short*)(L + L_TAB + off) = (unsigned short)(pk(kst, 0.f) & 0xffffu);
        if (l == 0) __hip_atomic_fetch_add((LAS unsigned*)(L + L_KSC), 1u, __ATOMIC_RELAXED, __HIP_MEMORY_SCOPE_WORKGROUP);
      }
#pragma unroll
      for (int i = 0; i < 3; ++i) {
#pragma unroll
        for (int s = 0; s < 3; ++s)
#pragma unroll
          for (int e = 0; e < 4; ++e) { own[i][2 * e] += bflo(pin[s][i][e]); own[i][2 * e + 1] += bfhi(pin[s][i][e]); }
        u32x4 val = {ne(pk(own[i][0], own[i][1])), ne(pk(own[i][2], own[i][3])), ne(pk(own[i][4], own[i][5])), ne(pk(own[i][6], own[i][7]))};
        __builtin_amdgcn_raw_buffer_store_b128(val, sW, l * 16 + finslot(mh, 3 * sc + i), 0, 16);
      }
      STAMP(5);
      {
        const unsigned ha = finslot(l & 1, 3 * ((l >> 1) & 3) + 2) + 1020;
        for (unsigned it = 0; it < SPIN_MAX; ++it) {
          asm volatile("" ::: "memory");
          const unsigned x = __builtin_amdgcn_raw_buffer_load_b32(sW, ha, 0, 16);
          if (__all(x != EMPTY)) break;
          __builtin_amdgcn_s_sleep(POLL_SLEEP);
        }
      }
      asm volatile("" ::: "memory");
      STAMP(6);
      u32x4 fin[24];
      const unsigned fb = (unsigned)W_FIN + (unsigned)(head * 72 + g * 24) * 1024u;
      for (unsigned it = 0; it < SPIN_MAX; ++it) {
        asm volatile("" ::: "memory");
#pragma unroll
        for (int i = 0; i < 24; ++i) fin[i] = __builtin_amdgcn_raw_buffer_load_b128(sW, l * 16, fb + i * 1024, 16);
        unsigned m = 0;
#pragma unroll
        for (int i = 0; i < 24; ++i) m = max3u(max3u(m, fin[i][0], fin[i][1]), fin[i][2], fin[i][3]);
        if (__all(m != EMPTY)) break;
      }
      asm volatile("" ::: "memory");
      for (unsigned it = 0; it < SPIN_MAX; ++it) {
        if (__hip_atomic_load((LAS unsigned*)(L + L_CNT), __ATOMIC_RELAXED, __HIP_MEMORY_SCOPE_WORKGROUP) == 7u) break;
        __builtin_amdgcn_s_sleep(1);
      }
      asm volatile("" ::: "memory");
#pragma unroll
      for (int i = 0; i < 24; ++i) *(LAS u32x4*)(L + L_V + (g * 24 + i) * 1024 + l * 16) = fin[i];
      STAMP(7);
      BAR();
      storer();
    }
    DUMPTS();
    __builtin_amdgcn_s_waitcnt(0);
  }
}

extern "C" void kernel_launch(void* const* d_in, const int* in_sizes, int n_in, void* d_out, int out_size, void* d_ws, size_t ws_size,
                              hipStream_t stream) {
  performer_fused<<<256, 512, 0, stream>>>((const char*)d_in[0], (const char*)d_in[1], (const char*)d_in[2], (const char*)d_in[3], (char*)d_out,
                                           (char*)d_ws);
}
```

```cpp
#include <hip/hip_runtime.h>
#include <hip/hip_bf16.h>
#include <stdint.h>

typedef unsigned long long u64;
typedef __attribute__((ext_vector_type(8))) __bf16 bf16x8;
typedef __attribute__((ext_vector_type(2))) __bf16 bf16x2;
typedef __attribute__((ext_vector_type(2))) float f32x2;
typedef __attribute__((ext_vector_type(16))) float f32x16;
typedef __attribute__((ext_vector_type(4))) unsigned u32x4;
typedef __attribute__((ext_vector_type(2))) unsigned u32x2;
typedef __attribute__((ext_vector_type(4))) short s16x4;
#define LAS __attribute__((address_space(3)))
typedef LAS char* lptr;

#define NT_IN 3
#define NT_Q 3
#define XST 16
#define XLD 19
#define NT_OUT 19
#define TS 0

constexpr int ROWB = 384;
constexpr int TILE = 12288;
constexpr int L_K = 0, L_V = 7 * TILE, L_X = L_V + 5 * TILE, L_BIAS = L_X + TILE, L_TAB = L_BIAS + 512, L_CNT = L_TAB + 512, L_NON = L_CNT + 8,
              L_FLG = L_CNT + 16, L_KSC = L_CNT + 32, L_SF = L_CNT + 48, L_FR = L_CNT + 64, L_Q0 = L_CNT + 80, L_DRN = L_CNT + 96, L_END = L_CNT + 112;
constexpr size_t W_P1 = 0;
constexpr size_t W_FIN = 32ull * 2 * 4 * 3 * 12 * 1024;
constexpr size_t W_KS = W_FIN + 32ull * 72 * 1024;
constexpr size_t W_INB = W_KS + 32ull * 8 * 3 * 128;
constexpr size_t W_HEL = W_INB + 256ull * 3 * 128;
constexpr size_t W_TS = W_HEL + 4096;
constexpr unsigned SPIN_MAX = 1u << 22;

__device__ __forceinline__ f32x16 mfma(u32x4 a, u32x4 b, f32x16 c) {
  return __builtin_amdgcn_mfma_f32_32x32x16_bf16(__builtin_bit_cast(bf16x8, a), __builtin_bit_cast(bf16x8, b), c, 0, 0, 0);
}
__device__ __forceinline__ unsigned pk(float a, float b) {
  f32x2 v = {a, b};
  return __builtin_bit_cast(unsigned, __builtin_convertvector(v, bf16x2));
}
__device__ __forceinline__ float dot2(unsigned a, unsigned b, float c) {
  return __builtin_amdgcn_fdot2_f32_bf16(__builtin_bit_cast(bf16x2, a), __builtin_bit_cast(bf16x2, b), c, false);
}
__device__ __forceinline__ u32x4 ld128(lptr p) { return *(LAS u32x4*)p; }
__device__ __forceinline__ u32x2 ldtr(lptr p) { return __builtin_bit_cast(u32x2, __builtin_amdgcn_ds_read_tr16_b64_v4i16((LAS s16x4*)p)); }
__device__ __forceinline__ float bflo(unsigned x) { return __uint_as_float(x << 16); }
__device__ __forceinline__ float bfhi(unsigned x) { return __uint_as_float(x & 0xffff0000u); }
__device__ __forceinline__ float halfsum(float x) {
  auto r = __builtin_amdgcn_permlane32_swap(__float_as_uint(x), __float_as_uint(x), false, false);
  return __uint_as_float(r[0]) + __uint_as_float(r[1]);
}
template <int NT>
__device__ __forceinline__ void dma(unsigned ldsaddr, unsigned voff, __amdgpu_buffer_rsrc_t srd, unsigned soff) {
  if constexpr (NT == 1)
    asm volatile("s_mov_b32 m0, %0\n\ts_nop 0\n\tbuffer_load_dwordx4 %1, %2, %3 offen nt lds" ::"s"(ldsaddr), "v"(voff), "s"(srd), "s"(soff) : "memory");
  else if constexpr (NT == 2)
    asm volatile("s_mov_b32 m0, %0\n\ts_nop 0\n\tbuffer_load_dwordx4 %1, %2, %3 offen sc1 nt lds" ::"s"(ldsaddr), "v"(voff), "s"(srd), "s"(soff) : "memory");
  else if constexpr (NT == 3)
    asm volatile("s_mov_b32 m0, %0\n\ts_nop 0\n\tbuffer_load_dwordx4 %1, %2, %3 offen sc0 sc1 nt lds" ::"s"(ldsaddr), "v"(voff), "s"(srd), "s"(soff) : "memory");
  else
    asm volatile("s_mov_b32 m0, %0\n\ts_nop 0\n\tbuffer_load_dwordx4 %1, %2, %3 offen lds" ::"s"(ldsaddr), "v"(voff), "s"(srd), "s"(soff) : "memory");
}
template <int V> struct IC { static constexpr int v = V; };
template <int I, int N, class F> __device__ __forceinline__ void sfor(F&& f) { if constexpr (I < N) { f(IC<I>{}); sfor<I + 1, N>(f); } }
#define SB() __builtin_amdgcn_sched_barrier(0)
#define G1PRIO 1
#define Q1AUX 0
#define DRAINW 3u
#define G1P 0
#define NS 8
#define KSV 1
#define QR0 3
#define QRN 6
#define POLL_SLEEP 2
#define PUBR 13
constexpr unsigned EMPTY = 0xffffffffu;
__device__ __forceinline__ unsigned ne(unsigned x) { return x < EMPTY - 1u ? x : EMPTY - 1u; }
__device__ __forceinline__ unsigned max3u(unsigned a, unsigned b, unsigned c) { const unsigned m = a > b ? a : b; return m > c ? m : c; }
#define WAITV(n) asm volatile("s_waitcnt vmcnt(" #n ")" ::: "memory")
#define WAITL() asm volatile("s_waitcnt lgkmcnt(0)" ::: "memory")
#define BAR() do { asm volatile("s_waitcnt lgkmcnt(0)" ::: "memory"); __builtin_amdgcn_s_barrier(); asm volatile("" ::: "memory"); } while (0)
typedef __attribute__((address_space(1))) u64 gu64;
__device__ __forceinline__ u64 ald(const char* p) { return __hip_atomic_load((gu64*)p, __ATOMIC_RELAXED, __HIP_MEMORY_SCOPE_AGENT); }
__device__ __forceinline__ void ast(char* p, u64 v) { __hip_atomic_store((gu64*)p, v, __ATOMIC_RELAXED, __HIP_MEMORY_SCOPE_AGENT); }
#define STAMP(i)
#define CSTAMP(i)
#define DUMPTS()
#define TNOW() 0ull

__device__ __forceinline__ void feat(const f32x16& acc, float c1, float nb, unsigned* o) {
#pragma unroll
  for (int i = 0; i < 8; ++i)
    o[i] = pk(__builtin_amdgcn_exp2f(__builtin_fmaf(acc[2 * i], c1, nb)), __builtin_amdgcn_exp2f(__builtin_fmaf(acc[2 * i + 1], c1, nb)));
}
__device__ __forceinline__ float rownorm(const u32x4* f) {
  float s0 = 0.f, s1 = 0.f;
#pragma unroll
  for (int i = 0; i < 12; ++i) {
    s0 = dot2(f[i][0], f[i][0], s0); s1 = dot2(f[i][1], f[i][1], s1);
    s0 = dot2(f[i][2], f[i][2], s0); s1 = dot2(f[i][3], f[i][3], s1);
  }
  return halfsum(s0 + s1);
}

__global__ __launch_bounds__(512, 2) void performer_fused(const char* __restrict__ q, const char* __restrict__ k, const char* __restrict__ v,
                                                          const char* __restrict__ proj, char* __restrict__ out, char* __restrict__ ws) {
  __shared__ __attribute__((aligned(16))) char lds_raw[L_END + (TS ? 1024 : 0)];
  lptr L = (lptr)lds_raw;
  const unsigned lbase = (unsigned)(uintptr_t)L;
  const int b = blockIdx.x;
  const int l = threadIdx.x & 63;
  const int w = __builtin_amdgcn_readfirstlane(threadIdx.x >> 6);
  const int head = (b & 7) * 4 + (b >> 6), i8 = (b >> 3) & 7, mh = i8 & 1, sc = i8 >> 1;
  const int h = l >> 5, r32 = l & 31;
  const float scale = 0.26864248f;
  const float LOG2E = 1.44269504089f;
  const float c1 = scale * LOG2E, cn = -0.5f * scale * scale * LOG2E, c0 = -3.79248125036f;
  const size_t hoff = (size_t)head * 2048 * ROWB;
  STAMP(0);

  unsigned fa[4];
#pragma unroll
  for (int kk = 0; kk < 4; ++kk) fa[kk] = r32 * ROWB + ((((2 * kk + h) ^ ((r32 >> 1) & 7))) << 4);
  auto ldfrags = [&](u32x4* f, unsigned base) {
#pragma unroll
    for (int ks = 0; ks < 12; ++ks) f[ks] = ld128(L + base + fa[ks & 3] + (ks >> 2) * 128);
  };

  unsigned vo[3];
#pragma unroll
  for (int p = 0; p < 3; ++p) { int Lc = p * 64 + l, row = Lc / 24, c = Lc % 24; vo[p] = row * ROWB + ((c ^ (row >> 1)) << 4); }
  auto issue12 = [&](auto nt, unsigned dst, __amdgpu_buffer_rsrc_t srd, unsigned soff) {
#pragma unroll
    for (int a = 0; a < 4; ++a)
#pragma unroll
      for (int p = 0; p < 3; ++p) dma<decltype(nt)::v>(dst + a * 3072 + p * 1024, vo[p] ^ ((a & 1) << 6), srd, soff + a * 3072);
  };
  const __amdgpu_buffer_rsrc_t sQ = __builtin_amdgcn_make_buffer_rsrc((void*)(q + hoff + (size_t)i8 * 256 * ROWB), 0, 256 * ROWB, 0x00020000);
  u64 tprev = 0, acc_c = 0, acc_v = 0, acc_b = 0;
#define RCLOSE(r, waits) do { WAITL(); const u64 ta = TNOW(); waits; const u64 tb = TNOW(); BAR(); const u64 tc = TNOW(); \
    if (TS >= 2 && (r) >= 2 && (r) <= 11) { acc_c += ta - tprev; acc_v += tb - ta; acc_b += tc - tb; } tprev = tc; } while (0)
#define DUMPACC()

  if (w < 4) {
    const __amdgpu_buffer_rsrc_t sK = __builtin_amdgcn_make_buffer_rsrc((void*)(k + hoff + (size_t)sc * 512 * ROWB), 0, 512 * ROWB, 0x00020000);
    const __amdgpu_buffer_rsrc_t sP = __builtin_amdgcn_make_buffer_rsrc((void*)proj, 0, 192 * ROWB, 0x00020000);
    {
      const unsigned par = (w & 1) << 6;
#pragma unroll
      for (int t = 0; t < 3; ++t)
#pragma unroll
        for (int p = 0; p < 3; ++p) dma<0>(lbase + L_K + (3 + t) * TILE + w * 3072 + p * 1024, vo[p] ^ par, sP, (3 * mh + t) * TILE + w * 3072);
#pragma unroll
      for (int t = 0; t < 3; ++t)
#pragma unroll
        for (int p = 0; p < 3; ++p) dma<NT_IN>(lbase + L_K + t * TILE + w * 3072 + p * 1024, vo[p] ^ par, sK, t * TILE + w * 3072);
    }
    WAITV(6);
    BAR();
    u32x4 pf[12], kf[12];
    if (w < 3) {
      ldfrags(pf, L_K + (3 + w) * TILE);
      ldfrags(kf, L_K);
      WAITV(0);
      BAR();
      STAMP(1);
      unsigned xw[4];
#pragma unroll
      for (int qq = 0; qq < 4; ++qq) xw[qq] = L_X + r32 * 192 + w * 64 + ((((2 * qq + h) ^ ((r32 >> 1) & 7))) << 3);
      if (G1P) __builtin_amdgcn_s_setprio(G1P);
      int nslot = 1;
      f32x16 acc[2];
      float nb = 0.f, nbn = 0.f;
      auto g1_round = [&](auto par, auto first, auto last, int r) {
        constexpr int P = decltype(par)::v;
        constexpr bool F = decltype(first)::v, Z = decltype(last)::v;
        const unsigned kb = L_K + nslot * TILE;
        constexpr unsigned xb = (P ^ 1) * 6144;
        nslot = nslot == 6 ? 0 : nslot + 1;
        unsigned o[8];
        auto step = [&](auto ic) {
          constexpr int i = decltype(ic)::v;
          const f32x16& pa = acc[P ^ 1];
          o[i] = pk(__builtin_amdgcn_exp2f(__builtin_fmaf(pa[2 * i], c1, nb)), __builtin_amdgcn_exp2f(__builtin_fmaf(pa[2 * i + 1], c1, nb)));
          if constexpr (i & 1) { u32x2 t = {o[i - 1], o[i]}; *(LAS u32x2*)(L + xb + xw[i >> 1]) = t; }
        };
        SB();
        if constexpr (!F) sfor<0, Z ? 8 : NS>([&](auto ic) { step(ic); SB(); });
        if constexpr (!Z) {
          nbn = *(LAS float*)(L + L_BIAS + (r & 3) * 128 + r32 * 4);
          sfor<0, 12>([&](auto ic) {
            constexpr int ks = decltype(ic)::v;
            acc[P] = mfma(pf[ks], kf[ks], ks == 0 ? (f32x16){} : acc[P]);
            kf[ks] = ld128(L + kb + fa[ks & 3] + (ks >> 2) * 128);
            if constexpr (!F && (ks & 1) == 0 && NS + ks / 2 < 8) step(IC<NS + ks / 2>{});
            SB();
          });
          nb = nbn;
        }
      };
#define G1CLOSE() do { asm volatile("s_waitcnt lgkmcnt(%0)" ::"n"(NS >= 8 ? 13 : 11 - 2 * (7 - NS)) : "memory"); __builtin_amdgcn_s_barrier(); asm volatile("" ::: "memory"); } while (0)
      g1_round(IC<0>{}, IC<1>{}, IC<0>{}, 0);
      G1CLOSE();
#pragma unroll 1
      for (int r = 1; r < 15; r += 2) {
        if (TS && r == 5) STAMP(10);
        if (TS && r == 13) STAMP(11);
        g1_round(IC<1>{}, IC<0>{}, IC<0>{}, r);
        G1CLOSE();
        g1_round(IC<0>{}, IC<0>{}, IC<0>{}, r + 1);
        G1CLOSE();
      }
      g1_round(IC<1>{}, IC<0>{}, IC<0>{}, 15);
      G1CLOSE();
      g1_round(IC<0>{}, IC<0>{}, IC<1>{}, 16);
      BAR();
      if (G1P) __builtin_amdgcn_s_setprio(0);
    } else {
      float mk[16];
      {
        const int dg = (h == ((r32 >> 2) & 1)) ? 4 * (r32 >> 3) + (r32 & 3) : -1;
#pragma unroll
        for (int i = 0; i < 16; ++i) mk[i] = i == dg ? 1.f : 0.f;
      }
      auto putbias = [&](const f32x16& a, int slot) {
        float s0 = 0.f, s1 = 0.f;
#pragma unroll
        for (int i = 0; i < 8; ++i) { s0 = __builtin_fmaf(a[2 * i], mk[2 * i], s0); s1 = __builtin_fmaf(a[2 * i + 1], mk[2 * i + 1], s1); }
        const float n = halfsum(s0 + s1);
        if (l < 32) *(LAS float*)(L + L_BIAS + slot * 128 + l * 4) = __builtin_fmaf(n, cn, c0);
      };
      ldfrags(kf, L_K);
      {
        f32x16 a = {};
#pragma unroll
        for (int ks = 0; ks < 12; ++ks) a = mfma(kf[ks], kf[ks], a);
        putbias(a, 0);
      }
      WAITV(0);
      BAR();
      int islot = 6, nslot = 2;
#pragma unroll 1
      for (int j = 3; j < 6; ++j) issue12(IC<NT_IN>{}, lbase + L_K + j * TILE, sK, j * TILE);
      ldfrags(kf, L_K + TILE);
      u32x4 kg[12];
      auto w3_round = [&](auto nt, const u32x4* cur, u32x4* nxt, __amdgpu_buffer_rsrc_t srd, unsigned soff, int r) {
        const unsigned kb = L_K + nslot * TILE;
        nslot = nslot == 6 ? 0 : nslot + 1;
        const unsigned dst = lbase + L_K + islot * TILE;
        islot = islot == 6 ? 0 : islot + 1;
        f32x16 a;
        sfor<0, 12>([&](auto ic) {
          constexpr int n = decltype(ic)::v, aa = n / 3, p = n % 3;
          a = mfma(cur[n], cur[n], n == 0 ? (f32x16){} : a);
          nxt[n] = ld128(L + kb + fa[n & 3] + (n >> 2) * 128);
          dma<decltype(nt)::v>(dst + aa * 3072 + p * 1024, vo[p] ^ ((aa & 1) << 6), srd, soff + aa * 3072);
          SB();
        });
        putbias(a, (r + 1) & 3);
        RCLOSE(r, if (r <= 12) WAITV(36));
      };
#pragma unroll 1
      for (int r = 0; r < 10; r += 2) {
        w3_round(IC<NT_IN>{}, kf, kg, sK, (r + 6) * TILE, r);
        w3_round(IC<NT_IN>{}, kg, kf, sK, (r + 7) * TILE, r + 1);
      }
#pragma unroll 1
      for (int r = 10; r < 16; r += 2) {
        w3_round(IC<0>{}, kf, kg, sP, (r - 10) * TILE, r);
        w3_round(IC<0>{}, kg, kf, sP, (r - 9) * TILE, r + 1);
      }
      RCLOSE(16, );
      WAITV(0);
      if (l == 0) *(LAS volatile unsigned*)(L + L_FLG) = 1u;
    }
    DUMPACC();
    for (unsigned it = 0; it < SPIN_MAX; ++it) {
      const unsigned f0 = *(LAS volatile unsigned*)(L + L_FLG), f1 = *(LAS volatile unsigned*)(L + L_FLG + 4);
      if (f0 != 0u && f1 > (unsigned)w) break;
      __builtin_amdgcn_s_sleep(1);
    }
    asm volatile("" ::: "memory");
    STAMP(2);
    CSTAMP(14);
    unsigned pq[2][6][8];
    const unsigned qslot = L_V + (1 + w) * TILE;
#pragma unroll
    for (int t = 0; t < 2; ++t) {
      if (t == 1) {
        for (unsigned it = 0; it < SPIN_MAX; ++it) {
          if (*(LAS volatile unsigned*)(L + L_FLG + 4) > 4u + (unsigned)w) break;
          __builtin_amdgcn_s_sleep(1);
        }
        asm volatile("" ::: "memory");
      }
      ldfrags(kf, qslot);
      const u32x4* qf = kf;
      u32x4 A[6];
#pragma unroll
      for (int i = 0; i < 6; ++i) A[i] = ld128(L + L_K + 2 * TILE + fa[i & 3] + (i >> 2) * 128);
      WAITL();
      if (t == 0) *(LAS volatile unsigned*)(L + L_Q0 + w * 4) = 1u;
      else if (l == 0) __hip_atomic_fetch_add((LAS unsigned*)(L + L_CNT), 1u, __ATOMIC_RELAXED, __HIP_MEMORY_SCOPE_WORKGROUP);
      STAMP(6 + 2 * t);
      const float nb = __builtin_fmaf(rownorm(qf), cn, c0);
      f32x16 acc[2];
      SB();
      sfor<0, 84>([&](auto ic) {
        constexpr int n = decltype(ic)::v, u = n / 12, ks = n % 12;
        if constexpr (n < 72) {
          acc[u & 1] = mfma(A[n % 6], qf[ks], ks == 0 ? (f32x16){} : acc[u & 1]);
          if constexpr (n + 6 < 72) { constexpr int m = n + 6, uu = m / 12, kk = m % 12; A[n % 6] = ld128(L + L_K + ((2 + uu) % 7) * TILE + fa[kk & 3] + (kk >> 2) * 128); }
        }
        if constexpr (u >= 1 && ks >= 2 && ks < 10) {
          constexpr int i = ks - 2;
          const f32x16& pa = acc[(u - 1) & 1];
          pq[t][u - 1][i] = pk(__builtin_amdgcn_exp2f(__builtin_fmaf(pa[2 * i], c1, nb)), __builtin_amdgcn_exp2f(__builtin_fmaf(pa[2 * i + 1], c1, nb)));
          asm volatile("" : "+v"(pq[t][u - 1][i]));
        }
        SB();
      });
    }
    STAMP(3);
    CSTAMP(15);
    float inv[2];
    {
      for (unsigned it = 0; it < SPIN_MAX; ++it) {
        if (*(LAS volatile unsigned*)(L + L_KSC) == 3u) break;
        __builtin_amdgcn_s_sleep(1);
      }
      asm volatile("" ::: "memory");
      f32x16 d0 = {}, d1 = {};
#pragma unroll
      for (int us = 0; us < 12; ++us) {
        const u32x4 a = ld128(L + L_TAB + h * 16 + us * 32);
        const unsigned* p0 = &pq[0][us >> 1][(us & 1) * 4];
        const unsigned* p1 = &pq[1][us >> 1][(us & 1) * 4];
        d0 = mfma(a, (u32x4){p0[0], p0[1], p0[2], p0[3]}, d0);
        d1 = mfma(a, (u32x4){p1[0], p1[1], p1[2], p1[3]}, d1);
      }
      inv[0] = 1.0f / (d0[0] + 1e-6f); inv[1] = 1.0f / (d1[0] + 1e-6f);
      asm volatile("" : "+v"(inv[0]), "+v"(inv[1]));
    }
    STAMP(7);
    BAR();
    STAMP(4);
    const unsigned sa = w * 16384 + r32 * 128 + ((r32 & 7) << 4) + h * 8;
    {
      f32x16 acc[2][2];
      u32x4 A[6];
      const unsigned kb = L_V + l * 16;
      unsigned fr = 0;
#pragma unroll
      for (int i = 0; i < 6; ++i) A[i] = ld128(L + kb + i * 1024);
      SB();
      sfor<0, 82>([&](auto ic) {
        constexpr int n = decltype(ic)::v, c = n / 12, us = n % 12, pb = c & 1;
        if constexpr (n < 72) {
          const unsigned* p0 = &pq[0][us >> 1][(us & 1) * 4];
          const unsigned* p1 = &pq[1][us >> 1][(us & 1) * 4];
          acc[pb][0] = mfma(A[n % 6], (u32x4){p0[0], p0[1], p0[2], p0[3]}, us == 0 ? (f32x16){} : acc[pb][0]);
          acc[pb][1] = mfma(A[n % 6], (u32x4){p1[0], p1[1], p1[2], p1[3]}, us == 0 ? (f32x16){} : acc[pb][1]);
          if constexpr (n + 6 < 72) A[n % 6] = ld128(L + kb + (n + 6) * 1024);
        }
        if constexpr (n == 54) fr = *(LAS volatile unsigned*)(L + L_FR + w * 4);
        if constexpr (n == 60) {
          while (fr == 0u) fr = *(LAS volatile unsigned*)(L + L_FR + w * 4);
        }
        if constexpr (c >= 1 && us >= 1 && us <= 8) {
          constexpr int dt = c - 1, e = us - 1, t = e >> 2, qq = e & 3;
          const f32x16& a = acc[pb ^ 1][t];
          u32x2 o = {pk(a[4 * qq] * inv[t], a[4 * qq + 1] * inv[t]), pk(a[4 * qq + 2] * inv[t], a[4 * qq + 3] * inv[t])};
          *(LAS u32x2*)(L + (sa ^ ((((dt & 1) * 4 + qq)) << 4)) + ((dt >> 1) & 1) * 8192 + t * 4096) = o;
          if constexpr ((dt & 1) && us == 8) *(LAS volatile unsigned*)(L + L_SF + w * 4) = (dt >> 1) + 1;
        }
        SB();
      });
    }
    STAMP(5);
    __builtin_amdgcn_s_waitcnt(0);
    STAMP(9);
    DUMPTS();
    __builtin_amdgcn_s_waitcnt(0);
  } else {
    const int g = w - 4;
    char* const hello = ws + W_HEL + (size_t)head * 64;
    auto storer = [&]() {
      const __amdgpu_buffer_rsrc_t sO = __builtin_amdgcn_make_buffer_rsrc((void*)(out + hoff + (size_t)i8 * 256 * ROWB), 0, 256 * ROWB, 0x00020000);
      const unsigned sr = g * 16384 + (l >> 3) * 128 + (((l & 7) ^ ((l >> 3) & 7)) << 4);
      const unsigned so = (g * 64 + (l >> 3)) * ROWB + (l & 7) * 16;
#pragma unroll
      for (int cp = 0; cp < 3; ++cp) {
        for (unsigned it = 0; it < SPIN_MAX; ++it) {
          if (*(LAS volatile unsigned*)(L + L_SF + g * 4) > (unsigned)cp) break;
          __builtin_amdgcn_s_sleep(2);
        }
        asm volatile("" ::: "memory");
        u32x4 val[8];
#pragma unroll
        for (int i = 0; i < 8; ++i) val[i] = ld128(L + sr + (cp & 1) * 8192 + i * 1024);
        WAITL();
        if (cp == 0) *(LAS volatile unsigned*)(L + L_FR + g * 4) = 1u;
#pragma unroll
        for (int i = 0; i < 8; ++i) __builtin_amdgcn_raw_buffer_store_b128(val[i], sO, so + i * 8 * ROWB + cp * 128, 0, NT_OUT);
      }
      __builtin_amdgcn_s_waitcnt(0);
      STAMP(9);
    };
    if (w == 7) {
      unsigned vv[3];
#pragma unroll
      for (int p = 0; p < 3; ++p) { int Lc = p * 64 + l, row = Lc / 24, c = Lc % 24; vv[p] = row * ROWB + ((c ^ (((row >> 1) & 1) << 2)) << 4); }
      const __amdgpu_buffer_rsrc_t sV = __builtin_amdgcn_make_buffer_rsrc((void*)(v + hoff + (size_t)sc * 512 * ROWB), 0, 512 * ROWB, 0x00020000);
      auto issueV = [&](int j, int slot) {
#pragma unroll
        for (int a = 0; a < 4; ++a)
#pragma unroll
          for (int p = 0; p < 3; ++p) dma<NT_IN>(lbase + L_V + slot * TILE + a * 3072 + p * 1024, vv[p], sV, j * TILE + a * 3072);
      };
      const u64 old = ald(hello + i8 * 8);
      const u64 n = old * 0x9E3779B97F4A7C15ull + 0x632BE59BD9B4E019ull;
      if (l == 0) {
        ast(hello + i8 * 8, n);
        *(LAS u64*)(L + L_NON) = n; *(LAS unsigned*)(L + L_CNT) = 0u; *(LAS u64*)(L + L_FLG) = 0ull; *(LAS unsigned*)(L + L_KSC) = 0u;
        *(LAS u32x4*)(L + L_SF) = (u32x4){0u, 0u, 0u, 0u}; *(LAS u32x4*)(L + L_FR) = (u32x4){0u, 0u, 0u, 0u};
        *(LAS u32x4*)(L + L_Q0) = (u32x4){0u, 0u, 0u, 0u}; *(LAS unsigned*)(L + L_DRN) = 0u;
      }
      WAITV(0);
#pragma unroll 1
      for (int j = 0; j < 3; ++j) issueV(j, j);
      BAR();
      BAR();
      int islot = 2;
#pragma unroll 1
      for (int r = 0; r <= 16; ++r) {
        if (r >= 1 && r <= 13) issueV(r + 2, islot);
        else if (r >= 14) issue12(IC<NT_Q>{}, lbase + L_V + (r - 13) * TILE, sQ, 2 * (r - 14) * TILE);
        islot = islot == 4 ? 0 : islot + 1;
        RCLOSE(r, WAITV(36));
      }
      issue12(IC<NT_Q>{}, lbase + L_V + 4 * TILE, sQ, 6 * TILE);
      DUMPACC();
      WAITV(36); if (l == 0) *(LAS volatile unsigned*)(L + L_FLG + 4) = 1u;
      WAITV(24); if (l == 0) *(LAS volatile unsigned*)(L + L_FLG + 4) = 2u;
      WAITV(12); if (l == 0) *(LAS volatile unsigned*)(L + L_FLG + 4) = 3u;
      WAITV(0);  if (l == 0) *(LAS volatile unsigned*)(L + L_FLG + 4) = 4u;
      if (DRAINW) {
        for (unsigned it = 0; it < SPIN_MAX; ++it) {
          if (*(LAS volatile unsigned*)(L + L_DRN) >= DRAINW) break;
          __builtin_amdgcn_s_sleep(1);
        }
      }
#pragma unroll 1
      for (int j = 0; j < 4; ++j) {
        for (unsigned it = 0; it < SPIN_MAX; ++it) {
          if (*(LAS volatile unsigned*)(L + L_Q0 + j * 4) != 0u) break;
          __builtin_amdgcn_s_sleep(1);
        }
        asm volatile("" ::: "memory");
        issue12(IC<NT_Q>{}, lbase + L_V + (1 + j) * TILE, sQ, (2 * j + 1) * TILE);
      }
      WAITV(36); if (l == 0) *(LAS volatile unsigned*)(L + L_FLG + 4) = 5u;
      WAITV(24); if (l == 0) *(LAS volatile unsigned*)(L + L_FLG + 4) = 6u;
      WAITV(12); if (l == 0) *(LAS volatile unsigned*)(L + L_FLG + 4) = 7u;
      WAITV(0);  if (l == 0) *(LAS volatile unsigned*)(L + L_FLG + 4) = 8u;
      BAR();
      storer();
    } else {
      BAR();
      BAR();
      const int blk = (l >> 4) & 1, q4 = (l & 15) >> 2, p4 = l & 3;
      unsigned xa[2], va[2];
#pragma unroll
      for (int t = 0; t < 2; ++t) xa[t] = L_X + (8 * h + 4 * t + q4) * 192 + ((((4 * blk + p4) ^ (4 * h + 2 * t + (q4 >> 1)))) << 3);
#pragma unroll
      for (int e = 0; e < 2; ++e) va[e] = L_V + (8 * h + q4) * ROWB + (2 * g + (e ^ ((q4 >> 1) & 1))) * 64 + blk * 32 + p4 * 8;
      f32x16 acc[2][3] = {};
      float ksum = 0.f, ksum2 = 0.f;
      u64 view = 0;
      u32x4 B[2][2], A[2][2][3];
      auto ldB = [&](unsigned vb) {
#pragma unroll
        for (int ks = 0; ks < 2; ++ks)
#pragma unroll
          for (int e = 0; e < 2; ++e) {
            const unsigned ad = va[e] + vb;
            u32x2 b0 = ldtr(L + ad + ks * 6144), b1 = ldtr(L + ad + ks * 6144 + 1536);
            B[ks][e] = (u32x4){b0[0], b0[1], b1[0], b1[1]};
          }
      };
      int vslot = 0;
      auto g2_round = [&](auto par, auto domul, auto doread) {
        constexpr int P = decltype(par)::v;
        constexpr bool M = decltype(domul)::v, R = decltype(doread)::v;
        SB();
        sfor<0, 12>([&](auto ic) {
          constexpr int n = decltype(ic)::v, ks = n / 6, j = (n % 6) >> 1, e = n & 1;
          if constexpr (M) acc[e][j] = mfma(A[P ^ 1][ks][j], B[ks][e], acc[e][j]);
          if constexpr (R && n < 6) {
            u32x2 a0 = ldtr(L + xa[0] + P * 6144 + (n / 3) * 3072 + (n % 3) * 64), a1 = ldtr(L + xa[1] + P * 6144 + (n / 3) * 3072 + (n % 3) * 64);
            A[P][n / 3][n % 3] = (u32x4){a0[0], a0[1], a1[0], a1[1]};
          }
          SB();
        });
        if constexpr (R) { ldB(vslot * TILE); vslot = vslot == 4 ? 0 : vslot + 1; }
        if constexpr (M) {
          auto sum = [&](auto jc) {
            constexpr int j = decltype(jc)::v;
#pragma unroll
            for (int ks = 0; ks < 2; ++ks)
#pragma unroll
              for (int i = 0; i < 4; ++i) {
                if (KSV) { ksum += bflo(A[P ^ 1][ks][j][i]); ksum2 += bfhi(A[P ^ 1][ks][j][i]); }
                else ksum = dot2(A[P ^ 1][ks][j][i], 0x3f803f80u, ksum);
              }
            asm volatile("" : "+v"(ksum), "+v"(ksum2));
          };
          if (g == 0) sum(IC<0>{}); else if (g == 1) sum(IC<1>{}); else sum(IC<2>{});
        }
      };
      const __amdgpu_buffer_rsrc_t sW = __builtin_amdgcn_make_buffer_rsrc((void*)ws, 0, (int)W_INB, 0x00020000);
      const unsigned p1own = (unsigned)W_P1 + (unsigned)((((head * 2 + mh) * 4 + sc) * 3 + g) * 12) * 1024u;
      auto finslot = [&](int pm, int f) {
        return (unsigned)W_FIN + (unsigned)(head * 72 + ((2 * g + f / 6) * 6 + 3 * pm + ((f % 6) >> 1)) * 2 + (f & 1)) * 1024u;
      };
      const unsigned ksown = (unsigned)W_KS + ((head * 8 + i8) * 3 + g) * 128;
      view = ald(hello + (l & 7) * 8);
      char* const inb = ws + W_INB;
      char* const myin = inb + ((size_t)(head * 8 + i8) * 3 + g) * 128;
      char* const tgt = inb + ((size_t)(head * 8 + (l & 7)) * 3 + g) * 128;
      const bool pub = l >= 8 && l < 16;
      if (G1PRIO) __builtin_amdgcn_s_setprio(G1PRIO);
      RCLOSE(0, );
      const u64 nown = *(LAS u64*)(L + L_NON);
      RCLOSE(1, );
      g2_round(IC<0>{}, IC<0>{}, IC<1>{});
      RCLOSE(2, );
#pragma unroll 1
      for (int r = 3; r <= 15; r += 2) {
        if (r <= 9) {
          const u32x4 e4 = {EMPTY, EMPTY, EMPTY, EMPTY};
          const int j0 = (r - 3) / 2 * 3;
#pragma unroll
          for (int i = 0; i < 3; ++i) {
            const unsigned a = r == 9 ? finslot(mh, 3 * sc + i) : p1own + (unsigned)(j0 < 3 * sc ? j0 + i : j0 + i + 3) * 1024u;
            __builtin_amdgcn_raw_buffer_store_b128(e4, sW, l * 16 + a, 0, XST);
          }
          if (r == 3 && l < 32) __builtin_amdgcn_raw_buffer_store_b32(EMPTY, sW, l * 4, ksown, XST);
        }
        if (r == PUBR) {
          WAITV(0);
          if (pub) ast(tgt + i8 * 8, view);
        }
        g2_round(IC<1>{}, IC<1>{}, IC<1>{});
        RCLOSE(r, );
        g2_round(IC<0>{}, IC<1>{}, IC<1>{});
        RCLOSE(r + 1, );
      }
      const char* const pa = pub ? hello + (l & 7) * 8 : myin + (l & 7) * 8;
      u64 x0 = ald(pa);
      g2_round(IC<1>{}, IC<1>{}, IC<1>{});
      g2_round(IC<0>{}, IC<1>{}, IC<0>{});
      DUMPACC();
      if (G1PRIO) __builtin_amdgcn_s_setprio(0);
      STAMP(2);
      WAITL();
      if (l == 0) __hip_atomic_fetch_add((LAS unsigned*)(L + L_CNT), 1u, __ATOMIC_RELAXED, __HIP_MEMORY_SCOPE_WORKGROUP);
      float own[3][8];
#pragma unroll
      for (int f = 0; f < 12; ++f) {
        const f32x16& a = acc[f / 6][(f % 6) >> 1];
        const int o = (f & 1) * 8;
        if (f / 3 != sc) {
          u32x4 val = {ne(pk(a[o], a[o + 1])), ne(pk(a[o + 2], a[o + 3])), ne(pk(a[o + 4], a[o + 5])), ne(pk(a[o + 6], a[o + 7]))};
          __builtin_amdgcn_raw_buffer_store_b128(val, sW, l * 16 + p1own + f * 1024, 0, XST);
        } else {
#pragma unroll
          for (int i = 0; i < 8; ++i) own[f % 3][i] = a[o + i];
        }
      }
      {
        const float ks = halfsum(ksum + ksum2);
        if (l < 32) __builtin_amdgcn_raw_buffer_store_b32(ne(__float_as_uint(ks)), sW, l * 4, ksown, XST);
      }
      WAITV(0);
      if (DRAINW && l == 0) __hip_atomic_fetch_add((LAS unsigned*)(L + L_DRN), 1u, __ATOMIC_RELAXED, __HIP_MEMORY_SCOPE_WORKGROUP);
      STAMP(3);
      for (unsigned it = 0; it < SPIN_MAX; ++it) {
        if (pub && x0 != view) { view = x0; ast(tgt + i8 * 8, view); }
        if (__all(l >= 8 || x0 == nown)) break;
        __builtin_amdgcn_s_sleep(POLL_SLEEP);
        x0 = ald(pa);
      }
      asm volatile("" ::: "memory");
      const u64 xl = ald(hello + (l & 7) * 8);
      float kst;
      for (unsigned it = 0; it < SPIN_MAX; ++it) {
        asm volatile("" ::: "memory");
        unsigned kw[4];
#pragma unroll
        for (int s = 0; s < 4; ++s)
          kw[s] = __builtin_amdgcn_raw_buffer_load_b32(sW, h * 384 + r32 * 4, (unsigned)W_KS + ((head * 8 + 2 * s) * 3 + g) * 128, XLD);
        kst = (__uint_as_float(kw[0]) + __uint_as_float(kw[1])) + (__uint_as_float(kw[2]) + __uint_as_float(kw[3]));
        if (__all(max3u(max3u(kw[0], kw[1], kw[2]), kw[3], 0u) != EMPTY)) break;
        __builtin_amdgcn_s_sleep(POLL_SLEEP);
      }
      asm volatile("" ::: "memory");
      if (pub && xl != view) { view = xl; ast(tgt + i8 * 8, view); }
      STAMP(4);
      u32x4 pin[3][3];
      for (unsigned it = 0; it < SPIN_MAX; ++it) {
        asm volatile("" ::: "memory");
#pragma unroll
        for (int s = 0; s < 3; ++s) {
          const int so = s + (s >= sc);
          const unsigned base = (unsigned)W_P1 + (unsigned)(((((head * 2 + mh) * 4 + so) * 3 + g) * 12) + 3 * sc) * 1024u;
#pragma unroll
          for (int i = 0; i < 3; ++i) pin[s][i] = __builtin_amdgcn_raw_buffer_load_b128(sW, l * 16, base + i * 1024, XLD);
        }
        unsigned m = 0;
#pragma unroll
        for (int s = 0; s < 3; ++s)
#pragma unroll
          for (int i = 0; i < 3; ++i) m = max3u(max3u(m, pin[s][i][0], pin[s][i][1]), pin[s][i][2], pin[s][i][3]);
        if (__all(m != EMPTY)) break;
      }
      asm volatile("" ::: "memory");
      {
        const int u = 3 * h + g;
        const unsigned off = ((u * 2 + (r32 >> 4)) * 2 + ((r32 >> 2) & 1)) * 16 + (((r32 >> 3) & 1) * 4 + (r32 & 3)) * 2;
        *(LAS unsigned short*)(L + L_TAB + off) = (unsigned short)(pk(kst, 0.f) & 0xffffu);
        if (l == 0) __hip_atomic_fetch_add((LAS unsigned*)(L + L_KSC), 1u, __ATOMIC_RELAXED, __HIP_MEMORY_SCOPE_WORKGROUP);
      }
#pragma unroll
      for (int i = 0; i < 3; ++i) {
#pragma unroll
        for (int s = 0; s < 3; ++s)
#pragma unroll
          for (int e = 0; e < 4; ++e) { own[i][2 * e] += bflo(pin[s][i][e]); own[i][2 * e + 1] += bfhi(pin[s][i][e]); }
        u32x4 val = {ne(pk(own[i][0], own[i][1])), ne(pk(own[i][2], own[i][3])), ne(pk(own[i][4], own[i][5])), ne(pk(own[i][6], own[i][7]))};
        __builtin_amdgcn_raw_buffer_store_b128(val, sW, l * 16 + finslot(mh, 3 * sc + i), 0, XST);
      }
      STAMP(5);
      {
        const unsigned ha = finslot(l & 1, 3 * ((l >> 1) & 3) + 2) + 1020;
        for (unsigned it = 0; it < SPIN_MAX; ++it) {
          asm volatile("" ::: "memory");
          const unsigned x = __builtin_amdgcn_raw_buffer_load_b32(sW, ha, 0, XLD);
          if (__all(x != EMPTY)) break;
          __builtin_amdgcn_s_sleep(POLL_SLEEP);
        }
      }
      asm volatile("" ::: "memory");
      STAMP(6);
      u32x4 fin[24];
      const unsigned fb = (unsigned)W_FIN + (unsigned)(head * 72 + g * 24) * 1024u;
      for (unsigned it = 0; it < SPIN_MAX; ++it) {
        asm volatile("" ::: "memory");
#pragma unroll
        for (int i = 0; i < 24; ++i) fin[i] = __builtin_amdgcn_raw_buffer_load_b128(sW, l * 16, fb + i * 1024, XLD);
        unsigned m = 0;
#pragma unroll
        for (int i = 0; i < 24; ++i) m = max3u(max3u(m, fin[i][0], fin[i][1]), fin[i][2], fin[i][3]);
        if (__all(m != EMPTY)) break;
      }
      asm volatile("" ::: "memory");
      for (unsigned it = 0; it < SPIN_MAX; ++it) {
        if (__hip_atomic_load((LAS unsigned*)(L + L_CNT), __ATOMIC_RELAXED, __HIP_MEMORY_SCOPE_WORKGROUP) == 7u) break;
        __builtin_amdgcn_s_sleep(1);
      }
      asm volatile("" ::: "memory");
#pragma unroll
      for (int i = 0; i < 24; ++i) *(LAS u32x4*)(L + L_V + (g * 24 + i) * 1024 + l * 16) = fin[i];
      STAMP(7);
      BAR();
      storer();
    }
    DUMPTS();
    __builtin_amdgcn_s_waitcnt(0);
  }
}

extern "C" void kernel_launch(void* const* d_in, const int* in_sizes, int n_in, void* d_out, int out_size, void* d_ws, size_t ws_size,
                              hipStream_t stream) {
  performer_fused<<<256, 512, 0, stream>>>((const char*)d_in[0], (const char*)d_in[1], (const char*)d_in[2], (const char*)d_in[3], (char*)d_out,
                                           (char*)d_ws);
}
```

```cpp
#include <hip/hip_runtime.h>
#include <hip/hip_bf16.h>
#include <stdint.h>

typedef unsigned long long u64;
typedef __attribute__((ext_vector_type(8))) __bf16 bf16x8;
typedef __attribute__((ext_vector_type(2))) __bf16 bf16x2;
typedef __attribute__((ext_vector_type(2))) float f32x2;
typedef __attribute__((ext_vector_type(16))) float f32x16;
typedef __attribute__((ext_vector_type(4))) unsigned u32x4;
typedef __attribute__((ext_vector_type(2))) unsigned u32x2;
typedef __attribute__((ext_vector_type(4))) short s16x4;
#define LAS __attribute__((address_space(3)))
typedef LAS char* lptr;

#define NT_IN 3
#define NT_Q 3
#define XST 16
#define XLD 16
#define Q2AUX 19
#define NT_OUT 19
#define TS 0

constexpr int ROWB = 384;
constexpr int TILE = 12288;
constexpr int L_K = 0, L_V = 7 * TILE, L_X = L_V + 5 * TILE, L_BIAS = L_X + TILE, L_TAB = L_BIAS + 512, L_CNT = L_TAB + 512, L_NON = L_CNT + 8,
              L_FLG = L_CNT + 16, L_KSC = L_CNT + 32, L_SF = L_CNT + 48, L_FR = L_CNT + 64, L_Q0 = L_CNT + 80, L_DRN = L_CNT + 96, L_END = L_CNT + 112;
constexpr size_t W_P1 = 0;
constexpr size_t W_FIN = 32ull * 2 * 4 * 3 * 12 * 1024;
constexpr size_t W_KS = W_FIN + 32ull * 72 * 1024;
constexpr size_t W_INB = W_KS + 32ull * 8 * 3 * 128;
constexpr size_t W_HEL = W_INB + 256ull * 3 * 128;
constexpr size_t W_TS = W_HEL + 4096;
constexpr unsigned SPIN_MAX = 1u << 22;

__device__ __forceinline__ f32x16 mfma(u32x4 a, u32x4 b, f32x16 c) {
  return __builtin_amdgcn_mfma_f32_32x32x16_bf16(__builtin_bit_cast(bf16x8, a), __builtin_bit_cast(bf16x8, b), c, 0, 0, 0);
}
__device__ __forceinline__ unsigned pk(float a, float b) {
  f32x2 v = {a, b};
  return __builtin_bit_cast(unsigned, __builtin_convertvector(v, bf16x2));
}
__device__ __forceinline__ float dot2(unsigned a, unsigned b, float c) {
  return __builtin_amdgcn_fdot2_f32_bf16(__builtin_bit_cast(bf16x2, a), __builtin_bit_cast(bf16x2, b), c, false);
}
__device__ __forceinline__ u32x4 ld128(lptr p) { return *(LAS u32x4*)p; }
__device__ __forceinline__ u32x2 ldtr(lptr p) { return __builtin_bit_cast(u32x2, __builtin_amdgcn_ds_read_tr16_b64_v4i16((LAS s16x4*)p)); }
__device__ __forceinline__ float bflo(unsigned x) { return __uint_as_float(x << 16); }
__device__ __forceinline__ float bfhi(unsigned x) { return __uint_as_float(x & 0xffff0000u); }
__device__ __forceinline__ float halfsum(float x) {
  auto r = __builtin_amdgcn_permlane32_swap(__float_as_uint(x), __float_as_uint(x), false, false);
  return __uint_as_float(r[0]) + __uint_as_float(r[1]);
}
template <int NT>
__device__ __forceinline__ void dma(unsigned ldsaddr, unsigned voff, __amdgpu_buffer_rsrc_t srd, unsigned soff) {
  if constexpr (NT == 1)
    asm volatile("s_mov_b32 m0, %0\n\ts_nop 0\n\tbuffer_load_dwordx4 %1, %2, %3 offen nt lds" ::"s"(ldsaddr), "v"(voff), "s"(srd), "s"(soff) : "memory");
  else if constexpr (NT == 2)
    asm volatile("s_mov_b32 m0, %0\n\ts_nop 0\n\tbuffer_load_dwordx4 %1, %2, %3 offen sc1 nt lds" ::"s"(ldsaddr), "v"(voff), "s"(srd), "s"(soff) : "memory");
  else if constexpr (NT == 3)
    asm volatile("s_mov_b32 m0, %0\n\ts_nop 0\n\tbuffer_load_dwordx4 %1, %2, %3 offen sc0 sc1 nt lds" ::"s"(ldsaddr), "v"(voff), "s"(srd), "s"(soff) : "memory");
  else
    asm volatile("s_mov_b32 m0, %0\n\ts_nop 0\n\tbuffer_load_dwordx4 %1, %2, %3 offen lds" ::"s"(ldsaddr), "v"(voff), "s"(srd), "s"(soff) : "memory");
}
template <int V> struct IC { static constexpr int v = V; };
template <int I, int N, class F> __device__ __forceinline__ void sfor(F&& f) { if constexpr (I < N) { f(IC<I>{}); sfor<I + 1, N>(f); } }
#define SB() __builtin_amdgcn_sched_barrier(0)
#define G1PRIO 1
#define Q1AUX 0
#define DRAINW 0u
#define G1P 0
#define NS 8
#define KSV 1
#define QR0 3
#define QRN 6
#define POLL_SLEEP 2
#define PUBR 13
constexpr unsigned EMPTY = 0xffffffffu;
__device__ __forceinline__ unsigned ne(unsigned x) { return x < EMPTY - 1u ? x : EMPTY - 1u; }
__device__ __forceinline__ unsigned max3u(unsigned a, unsigned b, unsigned c) { const unsigned m = a > b ? a : b; return m > c ? m : c; }
#define WAITV(n) asm volatile("s_waitcnt vmcnt(" #n ")" ::: "memory")
#define WAITL() asm volatile("s_waitcnt lgkmcnt(0)" ::: "memory")
#define BAR() do { asm volatile("s_waitcnt lgkmcnt(0)" ::: "memory"); __builtin_amdgcn_s_barrier(); asm volatile("" ::: "memory"); } while (0)
typedef __attribute__((address_space(1))) u64 gu64;
__device__ __forceinline__ u64 ald(const char* p) { return __hip_atomic_load((gu64*)p, __ATOMIC_RELAXED, __HIP_MEMORY_SCOPE_AGENT); }
__device__ __forceinline__ void ast(char* p, u64 v) { __hip_atomic_store((gu64*)p, v, __ATOMIC_RELAXED, __HIP_MEMORY_SCOPE_AGENT); }
#define STAMP(i)
#define CSTAMP(i)
#define DUMPTS()
#define TNOW() 0ull

__device__ __forceinline__ void feat(const f32x16& acc, float c1, float nb, unsigned* o) {
#pragma unroll
  for (int i = 0; i < 8; ++i)
    o[i] = pk(__builtin_amdgcn_exp2f(__builtin_fmaf(acc[2 * i], c1, nb)), __builtin_amdgcn_exp2f(__builtin_fmaf(acc[2 * i + 1], c1, nb)));
}
__device__ __forceinline__ float rownorm(const u32x4* f) {
  float s0 = 0.f, s1 = 0.f;
#pragma unroll
  for (int i = 0; i < 12; ++i) {
    s0 = dot2(f[i][0], f[i][0], s0); s1 = dot2(f[i][1], f[i][1], s1);
    s0 = dot2(f[i][2], f[i][2], s0); s1 = dot2(f[i][3], f[i][3], s1);
  }
  return halfsum(s0 + s1);
}

__global__ __launch_bounds__(512, 2) void performer_fused(const char* __restrict__ q, const char* __restrict__ k, const char* __restrict__ v,
                                                          const char* __restrict__ proj, char* __restrict__ out, char* __restrict__ ws) {
  __shared__ __attribute__((aligned(16))) char lds_raw[L_END + (TS ? 1024 : 0)];
  lptr L = (lptr)lds_raw;
  const unsigned lbase = (unsigned)(uintptr_t)L;
  const int b = blockIdx.x;
  const int l = threadIdx.x & 63;
  const int w = __builtin_amdgcn_readfirstlane(threadIdx.x >> 6);
  const int head = (b & 7) * 4 + (b >> 6), i8 = (b >> 3) & 7, mh = i8 & 1, sc = i8 >> 1;
  const int h = l >> 5, r32 = l & 31;
  const float scale = 0.26864248f;
  const float LOG2E = 1.44269504089f;
  const float c1 = scale * LOG2E, cn = -0.5f * scale * scale * LOG2E, c0 = -3.79248125036f;
  const size_t hoff = (size_t)head * 2048 * ROWB;
  STAMP(0);

  unsigned fa[4];
#pragma unroll
  for (int kk = 0; kk < 4; ++kk) fa[kk] = r32 * ROWB + ((((2 * kk + h) ^ ((r32 >> 1) & 7))) << 4);
  auto ldfrags = [&](u32x4* f, unsigned base) {
#pragma unroll
    for (int ks = 0; ks < 12; ++ks) f[ks] = ld128(L + base + fa[ks & 3] + (ks >> 2) * 128);
  };

  unsigned vo[3];
#pragma unroll
  for (int p = 0; p < 3; ++p) { int Lc = p * 64 + l, row = Lc / 24, c = Lc % 24; vo[p] = row * ROWB + ((c ^ (row >> 1)) << 4); }
  auto issue12 = [&](auto nt, unsigned dst, __amdgpu_buffer_rsrc_t srd, unsigned soff) {
#pragma unroll
    for (int a = 0; a < 4; ++a)
#pragma unroll
      for (int p = 0; p < 3; ++p) dma<decltype(nt)::v>(dst + a * 3072 + p * 1024, vo[p] ^ ((a & 1) << 6), srd, soff + a * 3072);
  };
  const __amdgpu_buffer_rsrc_t sQ = __builtin_amdgcn_make_buffer_rsrc((void*)(q + hoff + (size_t)i8 * 256 * ROWB), 0, 256 * ROWB, 0x00020000);
  u64 tprev = 0, acc_c = 0, acc_v = 0, acc_b = 0;
#define RCLOSE(r, waits) do { WAITL(); const u64 ta = TNOW(); waits; const u64 tb = TNOW(); BAR(); const u64 tc = TNOW(); \
    if (TS >= 2 && (r) >= 2 && (r) <= 11) { acc_c += ta - tprev; acc_v += tb - ta; acc_b += tc - tb; } tprev = tc; } while (0)
#define DUMPACC()

  if (w < 4) {
    const __amdgpu_buffer_rsrc_t sK = __builtin_amdgcn_make_buffer_rsrc((void*)(k + hoff + (size_t)sc * 512 * ROWB), 0, 512 * ROWB, 0x00020000);
    const __amdgpu_buffer_rsrc_t sP = __builtin_amdgcn_make_buffer_rsrc((void*)proj, 0, 192 * ROWB, 0x00020000);
    {
      const unsigned par = (w & 1) << 6;
#pragma unroll
      for (int t = 0; t < 3; ++t)
#pragma unroll
        for (int p = 0; p < 3; ++p) dma<0>(lbase + L_K + (3 + t) * TILE + w * 3072 + p * 1024, vo[p] ^ par, sP, (3 * mh + t) * TILE + w * 3072);
#pragma unroll
      for (int t = 0; t < 3; ++t)
#pragma unroll
        for (int p = 0; p < 3; ++p) dma<NT_IN>(lbase + L_K + t * TILE + w * 3072 + p * 1024, vo[p] ^ par, sK, t * TILE + w * 3072);
    }
    WAITV(6);
    BAR();
    u32x4 pf[12], kf[12];
    if (w < 3) {
      ldfrags(pf, L_K + (3 + w) * TILE);
      ldfrags(kf, L_K);
      WAITV(0);
      BAR();
      STAMP(1);
      unsigned xw[4];
#pragma unroll
      for (int qq = 0; qq < 4; ++qq) xw[qq] = L_X + r32 * 192 + w * 64 + ((((2 * qq + h) ^ ((r32 >> 1) & 7))) << 3);
      if (G1P) __builtin_amdgcn_s_setprio(G1P);
      int nslot = 1;
      f32x16 acc[2];
      float nb = 0.f, nbn = 0.f;
      auto g1_round = [&](auto par, auto first, auto last, int r) {
        constexpr int P = decltype(par)::v;
        constexpr bool F = decltype(first)::v, Z = decltype(last)::v;
        const unsigned kb = L_K + nslot * TILE;
        constexpr unsigned xb = (P ^ 1) * 6144;
        nslot = nslot == 6 ? 0 : nslot + 1;
        unsigned o[8];
        auto step = [&](auto ic) {
          constexpr int i = decltype(ic)::v;
          const f32x16& pa = acc[P ^ 1];
          o[i] = pk(__builtin_amdgcn_exp2f(__builtin_fmaf(pa[2 * i], c1, nb)), __builtin_amdgcn_exp2f(__builtin_fmaf(pa[2 * i + 1], c1, nb)));
          if constexpr (i & 1) { u32x2 t = {o[i - 1], o[i]}; *(LAS u32x2*)(L + xb + xw[i >> 1]) = t; }
        };
        SB();
        if constexpr (!F) sfor<0, Z ? 8 : NS>([&](auto ic) { step(ic); SB(); });
        if constexpr (!Z) {
          nbn = *(LAS float*)(L + L_BIAS + (r & 3) * 128 + r32 * 4);
          sfor<0, 12>([&](auto ic) {
            constexpr int ks = decltype(ic)::v;
            acc[P] = mfma(pf[ks], kf[ks], ks == 0 ? (f32x16){} : acc[P]);
            kf[ks] = ld128(L + kb + fa[ks & 3] + (ks >> 2) * 128);
            if constexpr (!F && (ks & 1) == 0 && NS + ks / 2 < 8) step(IC<NS + ks / 2>{});
            SB();
          });
          nb = nbn;
        }
      };
#define G1CLOSE() do { asm volatile("s_waitcnt lgkmcnt(%0)" ::"n"(NS >= 8 ? 13 : 11 - 2 * (7 - NS)) : "memory"); __builtin_amdgcn_s_barrier(); asm volatile("" ::: "memory"); } while (0)
      g1_round(IC<0>{}, IC<1>{}, IC<0>{}, 0);
      G1CLOSE();
#pragma unroll 1
      for (int r = 1; r < 15; r += 2) {
        if (TS && r == 5) STAMP(10);
        if (TS && r == 13) STAMP(11);
        g1_round(IC<1>{}, IC<0>{}, IC<0>{}, r);
        G1CLOSE();
        g1_round(IC<0>{}, IC<0>{}, IC<0>{}, r + 1);
        G1CLOSE();
      }
      g1_round(IC<1>{}, IC<0>{}, IC<0>{}, 15);
      G1CLOSE();
      g1_round(IC<0>{}, IC<0>{}, IC<1>{}, 16);
      BAR();
      if (G1P) __builtin_amdgcn_s_setprio(0);
    } else {
      float mk[16];
      {
        const int dg = (h == ((r32 >> 2) & 1)) ? 4 * (r32 >> 3) + (r32 & 3) : -1;
#pragma unroll
        for (int i = 0; i < 16; ++i) mk[i] = i == dg ? 1.f : 0.f;
      }
      auto putbias = [&](const f32x16& a, int slot) {
        float s0 = 0.f, s1 = 0.f;
#pragma unroll
        for (int i = 0; i < 8; ++i) { s0 = __builtin_fmaf(a[2 * i], mk[2 * i], s0); s1 = __builtin_fmaf(a[2 * i + 1], mk[2 * i + 1], s1); }
        const float n = halfsum(s0 + s1);
        if (l < 32) *(LAS float*)(L + L_BIAS + slot * 128 + l * 4) = __builtin_fmaf(n, cn, c0);
      };
      ldfrags(kf, L_K);
      {
        f32x16 a = {};
#pragma unroll
        for (int ks = 0; ks < 12; ++ks) a = mfma(kf[ks], kf[ks], a);
        putbias(a, 0);
      }
      WAITV(0);
      BAR();
      int islot = 6, nslot = 2;
#pragma unroll 1
      for (int j = 3; j < 6; ++j) issue12(IC<NT_IN>{}, lbase + L_K + j * TILE, sK, j * TILE);
      ldfrags(kf, L_K + TILE);
      u32x4 kg[12];
      auto w3_round = [&](auto nt, const u32x4* cur, u32x4* nxt, __amdgpu_buffer_rsrc_t srd, unsigned soff, int r) {
        const unsigned kb = L_K + nslot * TILE;
        nslot = nslot == 6 ? 0 : nslot + 1;
        const unsigned dst = lbase + L_K + islot * TILE;
        islot = islot == 6 ? 0 : islot + 1;
        f32x16 a;
        sfor<0, 12>([&](auto ic) {
          constexpr int n = decltype(ic)::v, aa = n / 3, p = n % 3;
          a = mfma(cur[n], cur[n], n == 0 ? (f32x16){} : a);
          nxt[n] = ld128(L + kb + fa[n & 3] + (n >> 2) * 128);
          dma<decltype(nt)::v>(dst + aa * 3072 + p * 1024, vo[p] ^ ((aa & 1) << 6), srd, soff + aa * 3072);
          SB();
        });
        putbias(a, (r + 1) & 3);
        RCLOSE(r, if (r <= 12) WAITV(36));
      };
#pragma unroll 1
      for (int r = 0; r < 10; r += 2) {
        w3_round(IC<NT_IN>{}, kf, kg, sK, (r + 6) * TILE, r);
        w3_round(IC<NT_IN>{}, kg, kf, sK, (r + 7) * TILE, r + 1);
      }
#pragma unroll 1
      for (int r = 10; r < 16; r += 2) {
        w3_round(IC<0>{}, kf, kg, sP, (r - 10) * TILE, r);
        w3_round(IC<0>{}, kg, kf, sP, (r - 9) * TILE, r + 1);
      }
      RCLOSE(16, );
      WAITV(0);
      if (l == 0) *(LAS volatile unsigned*)(L + L_FLG) = 1u;
    }
    DUMPACC();
    for (unsigned it = 0; it < SPIN_MAX; ++it) {
      const unsigned f0 = *(LAS volatile unsigned*)(L + L_FLG), f1 = *(LAS volatile unsigned*)(L + L_FLG + 4);
      if (f0 != 0u && f1 > (unsigned)w) break;
      __builtin_amdgcn_s_sleep(1);
    }
    asm volatile("" ::: "memory");
    STAMP(2);
    CSTAMP(14);
    unsigned pq[2][6][8];
    const unsigned qslot = L_V + (1 + w) * TILE;
#pragma unroll
    for (int t = 0; t < 2; ++t) {
      if (t == 1) {
        for (unsigned it = 0; it < SPIN_MAX; ++it) {
          if (*(LAS volatile unsigned*)(L + L_FLG + 4) > 4u + (unsigned)w) break;
          __builtin_amdgcn_s_sleep(1);
        }
        asm volatile("" ::: "memory");
      }
      ldfrags(kf, qslot);
      const u32x4* qf = kf;
      u32x4 A[6];
#pragma unroll
      for (int i = 0; i < 6; ++i) A[i] = ld128(L + L_K + 2 * TILE + fa[i & 3] + (i >> 2) * 128);
      WAITL();
      if (t == 0) *(LAS volatile unsigned*)(L + L_Q0 + w * 4) = 1u;
      else if (l == 0) __hip_atomic_fetch_add((LAS unsigned*)(L + L_CNT), 1u, __ATOMIC_RELAXED, __HIP_MEMORY_SCOPE_WORKGROUP);
      STAMP(6 + 2 * t);
      const float nb = __builtin_fmaf(rownorm(qf), cn, c0);
      f32x16 acc[2];
      SB();
      sfor<0, 84>([&](auto ic) {
        constexpr int n = decltype(ic)::v, u = n / 12, ks = n % 12;
        if constexpr (n < 72) {
          acc[u & 1] = mfma(A[n % 6], qf[ks], ks == 0 ? (f32x16){} : acc[u & 1]);
          if constexpr (n + 6 < 72) { constexpr int m = n + 6, uu = m / 12, kk = m % 12; A[n % 6] = ld128(L + L_K + ((2 + uu) % 7) * TILE + fa[kk & 3] + (kk >> 2) * 128); }
        }
        if constexpr (u >= 1 && ks >= 2 && ks < 10) {
          constexpr int i = ks - 2;
          const f32x16& pa = acc[(u - 1) & 1];
          pq[t][u - 1][i] = pk(__builtin_amdgcn_exp2f(__builtin_fmaf(pa[2 * i], c1, nb)), __builtin_amdgcn_exp2f(__builtin_fmaf(pa[2 * i + 1], c1, nb)));
          asm volatile("" : "+v"(pq[t][u - 1][i]));
        }
        SB();
      });
    }
    STAMP(3);
    CSTAMP(15);
    float inv[2];
    {
      for (unsigned it = 0; it < SPIN_MAX; ++it) {
        if (*(LAS volatile unsigned*)(L + L_KSC) == 3u) break;
        __builtin_amdgcn_s_sleep(1);
      }
      asm volatile("" ::: "memory");
      f32x16 d0 = {}, d1 = {};
#pragma unroll
      for (int us = 0; us < 12; ++us) {
        const u32x4 a = ld128(L + L_TAB + h * 16 + us * 32);
        const unsigned* p0 = &pq[0][us >> 1][(us & 1) * 4];
        const unsigned* p1 = &pq[1][us >> 1][(us & 1) * 4];
        d0 = mfma(a, (u32x4){p0[0], p0[1], p0[2], p0[3]}, d0);
        d1 = mfma(a, (u32x4){p1[0], p1[1], p1[2], p1[3]}, d1);
      }
      inv[0] = 1.0f / (d0[0] + 1e-6f); inv[1] = 1.0f / (d1[0] + 1e-6f);
      asm volatile("" : "+v"(inv[0]), "+v"(inv[1]));
    }
    STAMP(7);
    BAR();
    STAMP(4);
    const unsigned sa = w * 16384 + r32 * 128 + ((r32 & 7) << 4) + h * 8;
    {
      f32x16 acc[2][2];
      u32x4 A[6];
      const unsigned kb = L_V + l * 16;
      unsigned fr = 0;
#pragma unroll
      for (int i = 0; i < 6; ++i) A[i] = ld128(L + kb + i * 1024);
      SB();
      sfor<0, 82>([&](auto ic) {
        constexpr int n = decltype(ic)::v, c = n / 12, us = n % 12, pb = c & 1;
        if constexpr (n < 72) {
          const unsigned* p0 = &pq[0][us >> 1][(us & 1) * 4];
          const unsigned* p1 = &pq[1][us >> 1][(us & 1) * 4];
          acc[pb][0] = mfma(A[n % 6], (u32x4){p0[0], p0[1], p0[2], p0[3]}, us == 0 ? (f32x16){} : acc[pb][0]);
          acc[pb][1] = mfma(A[n % 6], (u32x4){p1[0], p1[1], p1[2], p1[3]}, us == 0 ? (f32x16){} : acc[pb][1]);
          if constexpr (n + 6 < 72) A[n % 6] = ld128(L + kb + (n + 6) * 1024);
        }
        if constexpr (n == 54) fr = *(LAS volatile unsigned*)(L + L_FR + w * 4);
        if constexpr (n == 60) {
          while (fr == 0u) fr = *(LAS volatile unsigned*)(L + L_FR + w * 4);
        }
        if constexpr (c >= 1 && us >= 1 && us <= 8) {
          constexpr int dt = c - 1, e = us - 1, t = e >> 2, qq = e & 3;
          const f32x16& a = acc[pb ^ 1][t];
          u32x2 o = {pk(a[4 * qq] * inv[t], a[4 * qq + 1] * inv[t]), pk(a[4 * qq + 2] * inv[t], a[4 * qq + 3] * inv[t])};
          *(LAS u32x2*)(L + (sa ^ ((((dt & 1) * 4 + qq)) << 4)) + ((dt >> 1) & 1) * 8192 + t * 4096) = o;
          if constexpr ((dt & 1) && us == 8) *(LAS volatile unsigned*)(L + L_SF + w * 4) = (dt >> 1) + 1;
        }
        SB();
      });
    }
    STAMP(5);
    __builtin_amdgcn_s_waitcnt(0);
    STAMP(9);
    DUMPTS();
    __builtin_amdgcn_s_waitcnt(0);
  } else {
    const int g = w - 4;
    char* const hello = ws + W_HEL + (size_t)head * 64;
    auto storer = [&]() {
      const __amdgpu_buffer_rsrc_t sO = __builtin_amdgcn_make_buffer_rsrc((void*)(out + hoff + (size_t)i8 * 256 * ROWB), 0, 256 * ROWB, 0x00020000);
      const unsigned sr = g * 16384 + (l >> 3) * 128 + (((l & 7) ^ ((l >> 3) & 7)) << 4);
      const unsigned so = (g * 64 + (l >> 3)) * ROWB + (l & 7) * 16;
#pragma unroll
      for (int cp = 0; cp < 3; ++cp) {
        for (unsigned it = 0; it < SPIN_MAX; ++it) {
          if (*(LAS volatile unsigned*)(L + L_SF + g * 4) > (unsigned)cp) break;
          __builtin_amdgcn_s_sleep(2);
        }
        asm volatile("" ::: "memory");
        u32x4 val[8];
#pragma unroll
        for (int i = 0; i < 8; ++i) val[i] = ld128(L + sr + (cp & 1) * 8192 + i * 1024);
        WAITL();
        if (cp == 0) *(LAS volatile unsigned*)(L + L_FR + g * 4) = 1u;
#pragma unroll
        for (int i = 0; i < 8; ++i) __builtin_amdgcn_raw_buffer_store_b128(val[i], sO, so + i * 8 * ROWB + cp * 128, 0, NT_OUT);
      }
      __builtin_amdgcn_s_waitcnt(0);
      STAMP(9);
    };
    if (w == 7) {
      unsigned vv[3];
#pragma unroll
      for (int p = 0; p < 3; ++p) { int Lc = p * 64 + l, row = Lc / 24, c = Lc % 24; vv[p] = row * ROWB + ((c ^ (((row >> 1) & 1) << 2)) << 4); }
      const __amdgpu_buffer_rsrc_t sV = __builtin_amdgcn_make_buffer_rsrc((void*)(v + hoff + (size_t)sc * 512 * ROWB), 0, 512 * ROWB, 0x00020000);
      auto issueV = [&](int j, int slot) {
#pragma unroll
        for (int a = 0; a < 4; ++a)
#pragma unroll
          for (int p = 0; p < 3; ++p) dma<NT_IN>(lbase + L_V + slot * TILE + a * 3072 + p * 1024, vv[p], sV, j * TILE + a * 3072);
      };
      const u64 old = ald(hello + i8 * 8);
      const u64 n = old * 0x9E3779B97F4A7C15ull + 0x632BE59BD9B4E019ull;
      if (l == 0) {
        ast(hello + i8 * 8, n);
        *(LAS u64*)(L + L_NON) = n; *(LAS unsigned*)(L + L_CNT) = 0u; *(LAS u64*)(L + L_FLG) = 0ull; *(LAS unsigned*)(L + L_KSC) = 0u;
        *(LAS u32x4*)(L + L_SF) = (u32x4){0u, 0u, 0u, 0u}; *(LAS u32x4*)(L + L_FR) = (u32x4){0u, 0u, 0u, 0u};
        *(LAS u32x4*)(L + L_Q0) = (u32x4){0u, 0u, 0u, 0u}; *(LAS unsigned*)(L + L_DRN) = 0u;
      }
      WAITV(0);
#pragma unroll 1
      for (int j = 0; j < 3; ++j) issueV(j, j);
      BAR();
      BAR();
      int islot = 2;
#pragma unroll 1
      for (int r = 0; r <= 9; ++r) {
        if (r >= 1) issueV(r + 2, islot);
        islot = islot == 4 ? 0 : islot + 1;
        RCLOSE(r, WAITV(36));
      }
      u32x4 q2[4][12];
      auto ldq2 = [&](auto jc) {
        constexpr int j = decltype(jc)::v;
#pragma unroll
        for (int a = 0; a < 4; ++a)
#pragma unroll
          for (int p = 0; p < 3; ++p)
            q2[j][3 * a + p] = __builtin_amdgcn_raw_buffer_load_b128(sQ, vo[p] ^ ((a & 1) << 6), (2 * j + 1) * TILE + a * 3072, Q2AUX);
      };
      ldq2(IC<0>{}); issueV(12, 2); RCLOSE(10, WAITV(48));
      ldq2(IC<1>{}); issueV(13, 3); RCLOSE(11, WAITV(60));
      ldq2(IC<2>{}); issueV(14, 4); RCLOSE(12, WAITV(60));
      ldq2(IC<3>{}); issueV(15, 0); RCLOSE(13, WAITV(60));
      issue12(IC<NT_Q>{}, lbase + L_V + 1 * TILE, sQ, 0 * TILE); RCLOSE(14, WAITV(60));
      issue12(IC<NT_Q>{}, lbase + L_V + 2 * TILE, sQ, 2 * TILE); RCLOSE(15, WAITV(48));
      issue12(IC<NT_Q>{}, lbase + L_V + 3 * TILE, sQ, 4 * TILE); RCLOSE(16, WAITV(36));
      issue12(IC<NT_Q>{}, lbase + L_V + 4 * TILE, sQ, 6 * TILE);
      DUMPACC();
      WAITV(36); if (l == 0) *(LAS volatile unsigned*)(L + L_FLG + 4) = 1u;
      WAITV(24); if (l == 0) *(LAS volatile unsigned*)(L + L_FLG + 4) = 2u;
      WAITV(12); if (l == 0) *(LAS volatile unsigned*)(L + L_FLG + 4) = 3u;
      WAITV(0);  if (l == 0) *(LAS volatile unsigned*)(L + L_FLG + 4) = 4u;
      sfor<0, 4>([&](auto jc) {
        constexpr int j = decltype(jc)::v;
        for (unsigned it = 0; it < SPIN_MAX; ++it) {
          if (*(LAS volatile unsigned*)(L + L_Q0 + j * 4) != 0u) break;
          __builtin_amdgcn_s_sleep(1);
        }
        asm volatile("" ::: "memory");
#pragma unroll
        for (int i = 0; i < 12; ++i) *(LAS u32x4*)(L + L_V + (1 + j) * TILE + i * 1024 + l * 16) = q2[j][i];
        WAITL();
        if (l == 0) *(LAS volatile unsigned*)(L + L_FLG + 4) = 5u + j;
      });
      BAR();
      storer();
    } else {
      BAR();
      BAR();
      const int blk = (l >> 4) & 1, q4 = (l & 15) >> 2, p4 = l & 3;
      unsigned xa[2], va[2];
#pragma unroll
      for (int t = 0; t < 2; ++t) xa[t] = L_X + (8 * h + 4 * t + q4) * 192 + ((((4 * blk + p4) ^ (4 * h + 2 * t + (q4 >> 1)))) << 3);
#pragma unroll
      for (int e = 0; e < 2; ++e) va[e] = L_V + (8 * h + q4) * ROWB + (2 * g + (e ^ ((q4 >> 1) & 1))) * 64 + blk * 32 + p4 * 8;
      f32x16 acc[2][3] = {};
      float ksum = 0.f, ksum2 = 0.f;
      u64 view = 0;
      u32x4 B[2][2], A[2][2][3];
      auto ldB = [&](unsigned vb) {
#pragma unroll
        for (int ks = 0; ks < 2; ++ks)
#pragma unroll
          for (int e = 0; e < 2; ++e) {
            const unsigned ad = va[e] + vb;
            u32x2 b0 = ldtr(L + ad + ks * 6144), b1 = ldtr(L + ad + ks * 6144 + 1536);
            B[ks][e] = (u32x4){b0[0], b0[1], b1[0], b1[1]};
          }
      };
      int vslot = 0;
      auto g2_round = [&](auto par, auto domul, auto doread) {
        constexpr int P = decltype(par)::v;
        constexpr bool M = decltype(domul)::v, R = decltype(doread)::v;
        SB();
        sfor<0, 12>([&](auto ic) {
          constexpr int n = decltype(ic)::v, ks = n / 6, j = (n % 6) >> 1, e = n & 1;
          if constexpr (M) acc[e][j] = mfma(A[P ^ 1][ks][j], B[ks][e], acc[e][j]);
          if constexpr (R && n < 6) {
            u32x2 a0 = ldtr(L + xa[0] + P * 6144 + (n / 3) * 3072 + (n % 3) * 64), a1 = ldtr(L + xa[1] + P * 6144 + (n / 3) * 3072 + (n % 3) * 64);
            A[P][n / 3][n % 3] = (u32x4){a0[0], a0[1], a1[0], a1[1]};
          }
          SB();
        });
        if constexpr (R) { ldB(vslot * TILE); vslot = vslot == 4 ? 0 : vslot + 1; }
        if constexpr (M) {
          auto sum = [&](auto jc) {
            constexpr int j = decltype(jc)::v;
#pragma unroll
            for (int ks = 0; ks < 2; ++ks)
#pragma unroll
              for (int i = 0; i < 4; ++i) {
                if (KSV) { ksum += bflo(A[P ^ 1][ks][j][i]); ksum2 += bfhi(A[P ^ 1][ks][j][i]); }
                else ksum = dot2(A[P ^ 1][ks][j][i], 0x3f803f80u, ksum);
              }
            asm volatile("" : "+v"(ksum), "+v"(ksum2));
          };
          if (g == 0) sum(IC<0>{}); else if (g == 1) sum(IC<1>{}); else sum(IC<2>{});
        }
      };
      const __amdgpu_buffer_rsrc_t sW = __builtin_amdgcn_make_buffer_rsrc((void*)ws, 0, (int)W_INB, 0x00020000);
      const unsigned p1own = (unsigned)W_P1 + (unsigned)((((head * 2 + mh) * 4 + sc) * 3 + g) * 12) * 1024u;
      auto finslot = [&](int pm, int f) {
        return (unsigned)W_FIN + (unsigned)(head * 72 + ((2 * g + f / 6) * 6 + 3 * pm + ((f % 6) >> 1)) * 2 + (f & 1)) * 1024u;
      };
      const unsigned ksown = (unsigned)W_KS + ((head * 8 + i8) * 3 + g) * 128;
      view = ald(hello + (l & 7) * 8);
      char* const inb = ws + W_INB;
      char* const myin = inb + ((size_t)(head * 8 + i8) * 3 + g) * 128;
      char* const tgt = inb + ((size_t)(head * 8 + (l & 7)) * 3 + g) * 128;
      const bool pub = l >= 8 && l < 16;
      if (G1PRIO) __builtin_amdgcn_s_setprio(G1PRIO);
      RCLOSE(0, );
      const u64 nown = *(LAS u64*)(L + L_NON);
      RCLOSE(1, );
      g2_round(IC<0>{}, IC<0>{}, IC<1>{});
      RCLOSE(2, );
#pragma unroll 1
      for (int r = 3; r <= 15; r += 2) {
        if (r <= 9) {
          const u32x4 e4 = {EMPTY, EMPTY, EMPTY, EMPTY};
          const int j0 = (r - 3) / 2 * 3;
#pragma unroll
          for (int i = 0; i < 3; ++i) {
            const unsigned a = r == 9 ? finslot(mh, 3 * sc + i) : p1own + (unsigned)(j0 < 3 * sc ? j0 + i : j0 + i + 3) * 1024u;
            __builtin_amdgcn_raw_buffer_store_b128(e4, sW, l * 16 + a, 0, XST);
          }
          if (r == 3 && l < 32) __builtin_amdgcn_raw_buffer_store_b32(EMPTY, sW, l * 4, ksown, XST);
        }
        if (r == PUBR) {
          WAITV(0);
          if (pub) ast(tgt + i8 * 8, view);
        }
        g2_round(IC<1>{}, IC<1>{}, IC<1>{});
        RCLOSE(r, );
        g2_round(IC<0>{}, IC<1>{}, IC<1>{});
        RCLOSE(r + 1, );
      }
      const char* const pa = pub ? hello + (l & 7) * 8 : myin + (l & 7) * 8;
      u64 x0 = ald(pa);
      g2_round(IC<1>{}, IC<1>{}, IC<1>{});
      g2_round(IC<0>{}, IC<1>{}, IC<0>{});
      DUMPACC();
      if (G1PRIO) __builtin_amdgcn_s_setprio(0);
      STAMP(2);
      WAITL();
      if (l == 0) __hip_atomic_fetch_add((LAS unsigned*)(L + L_CNT), 1u, __ATOMIC_RELAXED, __HIP_MEMORY_SCOPE_WORKGROUP);
      float own[3][8];
#pragma unroll
      for (int f = 0; f < 12; ++f) {
        const f32x16& a = acc[f / 6][(f % 6) >> 1];
        const int o = (f & 1) * 8;
        if (f / 3 != sc) {
          u32x4 val = {ne(pk(a[o], a[o + 1])), ne(pk(a[o + 2], a[o + 3])), ne(pk(a[o + 4], a[o + 5])), ne(pk(a[o + 6], a[o + 7]))};
          __builtin_amdgcn_raw_buffer_store_b128(val, sW, l * 16 + p1own + f * 1024, 0, XST);
        } else {
#pragma unroll
          for (int i = 0; i < 8; ++i) own[f % 3][i] = a[o + i];
        }
      }
      {
        const float ks = halfsum(ksum + ksum2);
        if (l < 32) __builtin_amdgcn_raw_buffer_store_b32(ne(__float_as_uint(ks)), sW, l * 4, ksown, XST);
      }
      WAITV(0);
      if (DRAINW && l == 0) __hip_atomic_fetch_add((LAS unsigned*)(L + L_DRN), 1u, __ATOMIC_RELAXED, __HIP_MEMORY_SCOPE_WORKGROUP);
      STAMP(3);
      for (unsigned it = 0; it < SPIN_MAX; ++it) {
        if (pub && x0 != view) { view = x0; ast(tgt + i8 * 8, view); }
        if (__all(l >= 8 || x0 == nown)) break;
        __builtin_amdgcn_s_sleep(POLL_SLEEP);
        x0 = ald(pa);
      }
      asm volatile("" ::: "memory");
      const u64 xl = ald(hello + (l & 7) * 8);
      float kst;
      for (unsigned it = 0; it < SPIN_MAX; ++it) {
        asm volatile("" ::: "memory");
        unsigned kw[4];
#pragma unroll
        for (int s = 0; s < 4; ++s)
          kw[s] = __builtin_amdgcn_raw_buffer_load_b32(sW, h * 384 + r32 * 4, (unsigned)W_KS + ((head * 8 + 2 * s) * 3 + g) * 128, XLD);
        kst = (__uint_as_float(kw[0]) + __uint_as_float(kw[1])) + (__uint_as_float(kw[2]) + __uint_as_float(kw[3]));
        if (__all(max3u(max3u(kw[0], kw[1], kw[2]), kw[3], 0u) != EMPTY)) break;
        __builtin_amdgcn_s_sleep(POLL_SLEEP);
      }
      asm volatile("" ::: "memory");
      if (pub && xl != view) { view = xl; ast(tgt + i8 * 8, view); }
      STAMP(4);
      u32x4 pin[3][3];
      for (unsigned it = 0; it < SPIN_MAX; ++it) {
        asm volatile("" ::: "memory");
#pragma unroll
        for (int s = 0; s < 3; ++s) {
          const int so = s + (s >= sc);
          const unsigned base = (unsigned)W_P1 + (unsigned)(((((head * 2 + mh) * 4 + so) * 3 + g) * 12) + 3 * sc) * 1024u;
#pragma unroll
          for (int i = 0; i < 3; ++i) pin[s][i] = __builtin_amdgcn_raw_buffer_load_b128(sW, l * 16, base + i * 1024, XLD);
        }
        unsigned m = 0;
#pragma unroll
        for (int s = 0; s < 3; ++s)
#pragma unroll
          for (int i = 0; i < 3; ++i) m = max3u(max3u(m, pin[s][i][0], pin[s][i][1]), pin[s][i][2], pin[s][i][3]);
        if (__all(m != EMPTY)) break;
      }
      asm volatile("" ::: "memory");
      {
        const int u = 3 * h + g;
        const unsigned off = ((u * 2 + (r32 >> 4)) * 2 + ((r32 >> 2) & 1)) * 16 + (((r32 >> 3) & 1) * 4 + (r32 & 3)) * 2;
        *(LAS unsigned short*)(L + L_TAB + off) = (unsigned short)(pk(kst, 0.f) & 0xffffu);
        if (l == 0) __hip_atomic_fetch_add((LAS unsigned*)(L + L_KSC), 1u, __ATOMIC_RELAXED, __HIP_MEMORY_SCOPE_WORKGROUP);
      }
#pragma unroll
      for (int i = 0; i < 3; ++i) {
#pragma unroll
        for (int s = 0; s < 3; ++s)
#pragma unroll
          for (int e = 0; e < 4; ++e) { own[i][2 * e] += bflo(pin[s][i][e]); own[i][2 * e + 1] += bfhi(pin[s][i][e]); }
        u32x4 val = {ne(pk(own[i][0], own[i][1])), ne(pk(own[i][2], own[i][3])), ne(pk(own[i][4], own[i][5])), ne(pk(own[i][6], own[i][7]))};
        __builtin_amdgcn_raw_buffer_store_b128(val, sW, l * 16 + finslot(mh, 3 * sc + i), 0, XST);
      }
      STAMP(5);
      {
        const unsigned ha = finslot(l & 1, 3 * ((l >> 1) & 3) + 2) + 1020;
        for (unsigned it = 0; it < SPIN_MAX; ++it) {
          asm volatile("" ::: "memory");
          const unsigned x = __builtin_amdgcn_raw_buffer_load_b32(sW, ha, 0, XLD);
          if (__all(x != EMPTY)) break;
          __builtin_amdgcn_s_sleep(POLL_SLEEP);
        }
      }
      asm volatile("" ::: "memory");
      STAMP(6);
      u32x4 fin[24];
      const unsigned fb = (unsigned)W_FIN + (unsigned)(head * 72 + g * 24) * 1024u;
      for (unsigned it = 0; it < SPIN_MAX; ++it) {
        asm volatile("" ::: "memory");
#pragma unroll
        for (int i = 0; i < 24; ++i) fin[i] = __builtin_amdgcn_raw_buffer_load_b128(sW, l * 16, fb + i * 1024, XLD);
        unsigned m = 0;
#pragma unroll
        for (int i = 0; i < 24; ++i) m = max3u(max3u(m, fin[i][0], fin[i][1]), fin[i][2], fin[i][3]);
        if (__all(m != EMPTY)) break;
      }
      asm volatile("" ::: "memory");
      for (unsigned it = 0; it < SPIN_MAX; ++it) {
        if (__hip_atomic_load((LAS unsigned*)(L + L_CNT), __ATOMIC_RELAXED, __HIP_MEMORY_SCOPE_WORKGROUP) == 7u) break;
        __builtin_amdgcn_s_sleep(1);
      }
      asm volatile("" ::: "memory");
#pragma unroll
      for (int i = 0; i < 24; ++i) *(LAS u32x4*)(L + L_V + (g * 24 + i) * 1024 + l * 16) = fin[i];
      STAMP(7);
      BAR();
      storer();
    }
    DUMPTS();
    __builtin_amdgcn_s_waitcnt(0);
  }
}

extern "C" void kernel_launch(void* const* d_in, const int* in_sizes, int n_in, void* d_out, int out_size, void* d_ws, size_t ws_size,
                              hipStream_t stream) {
  performer_fused<<<256, 512, 0, stream>>>((const char*)d_in[0], (const char*)d_in[1], (const char*)d_in[2], (const char*)d_in[3], (char*)d_out,
                                           (char*)d_ws);
}
```

```cpp
#include <hip/hip_runtime.h>
#include <hip/hip_bf16.h>
#include <stdint.h>

typedef unsigned long long u64;
typedef __attribute__((ext_vector_type(8))) __bf16 bf16x8;
typedef __attribute__((ext_vector_type(2))) __bf16 bf16x2;
typedef __attribute__((ext_vector_type(2))) float f32x2;
typedef __attribute__((ext_vector_type(16))) float f32x16;
typedef __attribute__((ext_vector_type(4))) unsigned u32x4;
typedef __attribute__((ext_vector_type(2))) unsigned u32x2;
typedef __attribute__((ext_vector_type(4))) short s16x4;
#define LAS __attribute__((address_space(3)))
typedef LAS char* lptr;

#define NT_IN 3
#define NT_Q 3
#define XST 16
#define XLD 16
#define Q2AUX 19
#define XPRIO 3
#define NT_OUT 19
#define TS 0

constexpr int ROWB = 384;
constexpr int TILE = 12288;
constexpr int L_K = 0, L_V = 7 * TILE, L_X = L_V + 5 * TILE, L_BIAS = L_X + TILE, L_TAB = L_BIAS + 512, L_CNT = L_TAB + 512, L_NON = L_CNT + 8,
              L_FLG = L_CNT + 16, L_KSC = L_CNT + 32, L_SF = L_CNT + 48, L_FR = L_CNT + 64, L_Q0 = L_CNT + 80, L_DRN = L_CNT + 96, L_END = L_CNT + 112;
constexpr size_t W_P1 = 0;
constexpr size_t W_FIN = 32ull * 2 * 4 * 3 * 12 * 1024;
constexpr size_t W_KS = W_FIN + 32ull * 72 * 1024;
constexpr size_t W_INB = W_KS + 32ull * 8 * 3 * 128;
constexpr size_t W_HEL = W_INB + 256ull * 3 * 128;
constexpr size_t W_TS = W_HEL + 4096;
constexpr unsigned SPIN_MAX = 1u << 22;

__device__ __forceinline__ f32x16 mfma(u32x4 a, u32x4 b, f32x16 c) {
  return __builtin_amdgcn_mfma_f32_32x32x16_bf16(__builtin_bit_cast(bf16x8, a), __builtin_bit_cast(bf16x8, b), c, 0, 0, 0);
}
__device__ __forceinline__ unsigned pk(float a, float b) {
  f32x2 v = {a, b};
  return __builtin_bit_cast(unsigned, __builtin_convertvector(v, bf16x2));
}
__device__ __forceinline__ float dot2(unsigned a, unsigned b, float c) {
  return __builtin_amdgcn_fdot2_f32_bf16(__builtin_bit_cast(bf16x2, a), __builtin_bit_cast(bf16x2, b), c, false);
}
__device__ __forceinline__ u32x4 ld128(lptr p) { return *(LAS u32x4*)p; }
__device__ __forceinline__ u32x2 ldtr(lptr p) { return __builtin_bit_cast(u32x2, __builtin_amdgcn_ds_read_tr16_b64_v4i16((LAS s16x4*)p)); }
__device__ __forceinline__ float bflo(unsigned x) { return __uint_as_float(x << 16); }
__device__ __forceinline__ float bfhi(unsigned x) { return __uint_as_float(x & 0xffff0000u); }
__device__ __forceinline__ float halfsum(float x) {
  auto r = __builtin_amdgcn_permlane32_swap(__float_as_uint(x), __float_as_uint(x), false, false);
  return __uint_as_float(r[0]) + __uint_as_float(r[1]);
}
template <int NT>
__device__ __forceinline__ void dma(unsigned ldsaddr, unsigned voff, __amdgpu_buffer_rsrc_t srd, unsigned soff) {
  if constexpr (NT == 1)
    asm volatile("s_mov_b32 m0, %0\n\ts_nop 0\n\tbuffer_load_dwordx4 %1, %2, %3 offen nt lds" ::"s"(ldsaddr), "v"(voff), "s"(srd), "s"(soff) : "memory");
  else if constexpr (NT == 2)
    asm volatile("s_mov_b32 m0, %0\n\ts_nop 0\n\tbuffer_load_dwordx4 %1, %2, %3 offen sc1 nt lds" ::"s"(ldsaddr), "v"(voff), "s"(srd), "s"(soff) : "memory");
  else if constexpr (NT == 3)
    asm volatile("s_mov_b32 m0, %0\n\ts_nop 0\n\tbuffer_load_dwordx4 %1, %2, %3 offen sc0 sc1 nt lds" ::"s"(ldsaddr), "v"(voff), "s"(srd), "s"(soff) : "memory");
  else
    asm volatile("s_mov_b32 m0, %0\n\ts_nop 0\n\tbuffer_load_dwordx4 %1, %2, %3 offen lds" ::"s"(ldsaddr), "v"(voff), "s"(srd), "s"(soff) : "memory");
}
template <int V> struct IC { static constexpr int v = V; };
template <int I, int N, class F> __device__ __forceinline__ void sfor(F&& f) { if constexpr (I < N) { f(IC<I>{}); sfor<I + 1, N>(f); } }
#define SB() __builtin_amdgcn_sched_barrier(0)
#define G1PRIO 1
#define Q1AUX 0
#define DRAINW 0u
#define G1P 0
#define NS 8
#define KSV 1
#define QR0 3
#define QRN 6
#define POLL_SLEEP 2
#define PUBR 13
constexpr unsigned EMPTY = 0xffffffffu;
__device__ __forceinline__ unsigned ne(unsigned x) { return x < EMPTY - 1u ? x : EMPTY - 1u; }
__device__ __forceinline__ unsigned max3u(unsigned a, unsigned b, unsigned c) { const unsigned m = a > b ? a : b; return m > c ? m : c; }
#define WAITV(n) asm volatile("s_waitcnt vmcnt(" #n ")" ::: "memory")
#define WAITL() asm volatile("s_waitcnt lgkmcnt(0)" ::: "memory")
#define BAR() do { asm volatile("s_waitcnt lgkmcnt(0)" ::: "memory"); __builtin_amdgcn_s_barrier(); asm volatile("" ::: "memory"); } while (0)
typedef __attribute__((address_space(1))) u64 gu64;
__device__ __forceinline__ u64 ald(const char* p) { return __hip_atomic_load((gu64*)p, __ATOMIC_RELAXED, __HIP_MEMORY_SCOPE_AGENT); }
__device__ __forceinline__ void ast(char* p, u64 v) { __hip_atomic_store((gu64*)p, v, __ATOMIC_RELAXED, __HIP_MEMORY_SCOPE_AGENT); }
#define STAMP(i)
#define CSTAMP(i)
#define DUMPTS()
#define TNOW() 0ull

__device__ __forceinline__ void feat(const f32x16& acc, float c1, float nb, unsigned* o) {
#pragma unroll
  for (int i = 0; i < 8; ++i)
    o[i] = pk(__builtin_amdgcn_exp2f(__builtin_fmaf(acc[2 * i], c1, nb)), __builtin_amdgcn_exp2f(__builtin_fmaf(acc[2 * i + 1], c1, nb)));
}
__device__ __forceinline__ float rownorm(const u32x4* f) {
  float s0 = 0.f, s1 = 0.f;
#pragma unroll
  for (int i = 0; i < 12; ++i) {
    s0 = dot2(f[i][0], f[i][0], s0); s1 = dot2(f[i][1], f[i][1], s1);
    s0 = dot2(f[i][2], f[i][2], s0); s1 = dot2(f[i][3], f[i][3], s1);
  }
  return halfsum(s0 + s1);
}

__global__ __launch_bounds__(512, 2) void performer_fused(const char* __restrict__ q, const char* __restrict__ k, const char* __restrict__ v,
                                                          const char* __restrict__ proj, char* __restrict__ out, char* __restrict__ ws) {
  __shared__ __attribute__((aligned(16))) char lds_raw[L_END + (TS ? 1024 : 0)];
  lptr L = (lptr)lds_raw;
  const unsigned lbase = (unsigned)(uintptr_t)L;
  const int b = blockIdx.x;
  const int l = threadIdx.x & 63;
  const int w = __builtin_amdgcn_readfirstlane(threadIdx.x >> 6);
  const int head = (b & 7) * 4 + (b >> 6), i8 = (b >> 3) & 7, mh = i8 & 1, sc = i8 >> 1;
  const int h = l >> 5, r32 = l & 31;
  const float scale = 0.26864248f;
  const float LOG2E = 1.44269504089f;
  const float c1 = scale * LOG2E, cn = -0.5f * scale * scale * LOG2E, c0 = -3.79248125036f;
  const size_t hoff = (size_t)head * 2048 * ROWB;
  STAMP(0);

  unsigned fa[4];
#pragma unroll
  for (int kk = 0; kk < 4; ++kk) fa[kk] = r32 * ROWB + ((((2 * kk + h) ^ ((r32 >> 1) & 7))) << 4);
  auto ldfrags = [&](u32x4* f, unsigned base) {
#pragma unroll
    for (int ks = 0; ks < 12; ++ks) f[ks] = ld128(L + base + fa[ks & 3] + (ks >> 2) * 128);
  };

  unsigned vo[3];
#pragma unroll
  for (int p = 0; p < 3; ++p) { int Lc = p * 64 + l, row = Lc / 24, c = Lc % 24; vo[p] = row * ROWB + ((c ^ (row >> 1)) << 4); }
  auto issue12 = [&](auto nt, unsigned dst, __amdgpu_buffer_rsrc_t srd, unsigned soff) {
#pragma unroll
    for (int a = 0; a < 4; ++a)
#pragma unroll
      for (int p = 0; p < 3; ++p) dma<decltype(nt)::v>(dst + a * 3072 + p * 1024, vo[p] ^ ((a & 1) << 6), srd, soff + a * 3072);
  };
  const __amdgpu_buffer_rsrc_t sQ = __builtin_amdgcn_make_buffer_rsrc((void*)(q + hoff + (size_t)i8 * 256 * ROWB), 0, 256 * ROWB, 0x00020000);
  u64 tprev = 0, acc_c = 0, acc_v = 0, acc_b = 0;
#define RCLOSE(r, waits) do { WAITL(); const u64 ta = TNOW(); waits; const u64 tb = TNOW(); BAR(); const u64 tc = TNOW(); \
    if (TS >= 2 && (r) >= 2 && (r) <= 11) { acc_c += ta - tprev; acc_v += tb - ta; acc_b += tc - tb; } tprev = tc; } while (0)
#define DUMPACC()

  if (w < 4) {
    const __amdgpu_buffer_rsrc_t sK = __builtin_amdgcn_make_buffer_rsrc((void*)(k + hoff + (size_t)sc * 512 * ROWB), 0, 512 * ROWB, 0x00020000);
    const __amdgpu_buffer_rsrc_t sP = __builtin_amdgcn_make_buffer_rsrc((void*)proj, 0, 192 * ROWB, 0x00020000);
    {
      const unsigned par = (w & 1) << 6;
#pragma unroll
      for (int t = 0; t < 3; ++t)
#pragma unroll
        for (int p = 0; p < 3; ++p) dma<0>(lbase + L_K + (3 + t) * TILE + w * 3072 + p * 1024, vo[p] ^ par, sP, (3 * mh + t) * TILE + w * 3072);
#pragma unroll
      for (int t = 0; t < 3; ++t)
#pragma unroll
        for (int p = 0; p < 3; ++p) dma<NT_IN>(lbase + L_K + t * TILE + w * 3072 + p * 1024, vo[p] ^ par, sK, t * TILE + w * 3072);
    }
    WAITV(6);
    BAR();
    u32x4 pf[12], kf[12];
    if (w < 3) {
      ldfrags(pf, L_K + (3 + w) * TILE);
      ldfrags(kf, L_K);
      WAITV(0);
      BAR();
      STAMP(1);
      unsigned xw[4];
#pragma unroll
      for (int qq = 0; qq < 4; ++qq) xw[qq] = L_X + r32 * 192 + w * 64 + ((((2 * qq + h) ^ ((r32 >> 1) & 7))) << 3);
      if (G1P) __builtin_amdgcn_s_setprio(G1P);
      int nslot = 1;
      f32x16 acc[2];
      float nb = 0.f, nbn = 0.f;
      auto g1_round = [&](auto par, auto first, auto last, int r) {
        constexpr int P = decltype(par)::v;
        constexpr bool F = decltype(first)::v, Z = decltype(last)::v;
        const unsigned kb = L_K + nslot * TILE;
        constexpr unsigned xb = (P ^ 1) * 6144;
        nslot = nslot == 6 ? 0 : nslot + 1;
        unsigned o[8];
        auto step = [&](auto ic) {
          constexpr int i = decltype(ic)::v;
          const f32x16& pa = acc[P ^ 1];
          o[i] = pk(__builtin_amdgcn_exp2f(__builtin_fmaf(pa[2 * i], c1, nb)), __builtin_amdgcn_exp2f(__builtin_fmaf(pa[2 * i + 1], c1, nb)));
          if constexpr (i & 1) { u32x2 t = {o[i - 1], o[i]}; *(LAS u32x2*)(L + xb + xw[i >> 1]) = t; }
        };
        SB();
        if constexpr (!F) sfor<0, Z ? 8 : NS>([&](auto ic) { step(ic); SB(); });
        if constexpr (!Z) {
          nbn = *(LAS float*)(L + L_BIAS + (r & 3) * 128 + r32 * 4);
          sfor<0, 12>([&](auto ic) {
            constexpr int ks = decltype(ic)::v;
            acc[P] = mfma(pf[ks], kf[ks], ks == 0 ? (f32x16){} : acc[P]);
            kf[ks] = ld128(L + kb + fa[ks & 3] + (ks >> 2) * 128);
            if constexpr (!F && (ks & 1) == 0 && NS + ks / 2 < 8) step(IC<NS + ks / 2>{});
            SB();
          });
          nb = nbn;
        }
      };
#define G1CLOSE() do { asm volatile("s_waitcnt lgkmcnt(%0)" ::"n"(NS >= 8 ? 13 : 11 - 2 * (7 - NS)) : "memory"); __builtin_amdgcn_s_barrier(); asm volatile("" ::: "memory"); } while (0)
      g1_round(IC<0>{}, IC<1>{}, IC<0>{}, 0);
      G1CLOSE();
#pragma unroll 1
      for (int r = 1; r < 15; r += 2) {
        if (TS && r == 5) STAMP(10);
        if (TS && r == 13) STAMP(11);
        g1_round(IC<1>{}, IC<0>{}, IC<0>{}, r);
        G1CLOSE();
        g1_round(IC<0>{}, IC<0>{}, IC<0>{}, r + 1);
        G1CLOSE();
      }
      g1_round(IC<1>{}, IC<0>{}, IC<0>{}, 15);
      G1CLOSE();
      g1_round(IC<0>{}, IC<0>{}, IC<1>{}, 16);
      BAR();
      if (G1P) __builtin_amdgcn_s_setprio(0);
    } else {
      float mk[16];
      {
        const int dg = (h == ((r32 >> 2) & 1)) ? 4 * (r32 >> 3) + (r32 & 3) : -1;
#pragma unroll
        for (int i = 0; i < 16; ++i) mk[i] = i == dg ? 1.f : 0.f;
      }
      auto putbias = [&](const f32x16& a, int slot) {
        float s0 = 0.f, s1 = 0.f;
#pragma unroll
        for (int i = 0; i < 8; ++i) { s0 = __builtin_fmaf(a[2 * i], mk[2 * i], s0); s1 = __builtin_fmaf(a[2 * i + 1], mk[2 * i + 1], s1); }
        const float n = halfsum(s0 + s1);
        if (l < 32) *(LAS float*)(L + L_BIAS + slot * 128 + l * 4) = __builtin_fmaf(n, cn, c0);
      };
      ldfrags(kf, L_K);
      {
        f32x16 a = {};
#pragma unroll
        for (int ks = 0; ks < 12; ++ks) a = mfma(kf[ks], kf[ks], a);
        putbias(a, 0);
      }
      WAITV(0);
      BAR();
      int islot = 6, nslot = 2;
#pragma unroll 1
      for (int j = 3; j < 6; ++j) issue12(IC<NT_IN>{}, lbase + L_K + j * TILE, sK, j * TILE);
      ldfrags(kf, L_K + TILE);
      u32x4 kg[12];
      auto w3_round = [&](auto nt, const u32x4* cur, u32x4* nxt, __amdgpu_buffer_rsrc_t srd, unsigned soff, int r) {
        const unsigned kb = L_K + nslot * TILE;
        nslot = nslot == 6 ? 0 : nslot + 1;
        const unsigned dst = lbase + L_K + islot * TILE;
        islot = islot == 6 ? 0 : islot + 1;
        f32x16 a;
        sfor<0, 12>([&](auto ic) {
          constexpr int n = decltype(ic)::v, aa = n / 3, p = n % 3;
          a = mfma(cur[n], cur[n], n == 0 ? (f32x16){} : a);
          nxt[n] = ld128(L + kb + fa[n & 3] + (n >> 2) * 128);
          dma<decltype(nt)::v>(dst + aa * 3072 + p * 1024, vo[p] ^ ((aa & 1) << 6), srd, soff + aa * 3072);
          SB();
        });
        putbias(a, (r + 1) & 3);
        RCLOSE(r, if (r <= 12) WAITV(36));
      };
#pragma unroll 1
      for (int r = 0; r < 10; r += 2) {
        w3_round(IC<NT_IN>{}, kf, kg, sK, (r + 6) * TILE, r);
        w3_round(IC<NT_IN>{}, kg, kf, sK, (r + 7) * TILE, r + 1);
      }
#pragma unroll 1
      for (int r = 10; r < 16; r += 2) {
        w3_round(IC<0>{}, kf, kg, sP, (r - 10) * TILE, r);
        w3_round(IC<0>{}, kg, kf, sP, (r - 9) * TILE, r + 1);
      }
      RCLOSE(16, );
      WAITV(0);
      if (l == 0) *(LAS volatile unsigned*)(L + L_FLG) = 1u;
    }
    DUMPACC();
    for (unsigned it = 0; it < SPIN_MAX; ++it) {
      const unsigned f0 = *(LAS volatile unsigned*)(L + L_FLG), f1 = *(LAS volatile unsigned*)(L + L_FLG + 4);
      if (f0 != 0u && f1 > (unsigned)w) break;
      __builtin_amdgcn_s_sleep(1);
    }
    asm volatile("" ::: "memory");
    STAMP(2);
    CSTAMP(14);
    unsigned pq[2][6][8];
    const unsigned qslot = L_V + (1 + w) * TILE;
#pragma unroll
    for (int t = 0; t < 2; ++t) {
      if (t == 1) {
        for (unsigned it = 0; it < SPIN_MAX; ++it) {
          if (*(LAS volatile unsigned*)(L + L_FLG + 4) > 4u + (unsigned)w) break;
          __builtin_amdgcn_s_sleep(1);
        }
        asm volatile("" ::: "memory");
      }
      ldfrags(kf, qslot);
      const u32x4* qf = kf;
      u32x4 A[6];
#pragma unroll
      for (int i = 0; i < 6; ++i) A[i] = ld128(L + L_K + 2 * TILE + fa[i & 3] + (i >> 2) * 128);
      WAITL();
      if (t == 0) *(LAS volatile unsigned*)(L + L_Q0 + w * 4) = 1u;
      else if (l == 0) __hip_atomic_fetch_add((LAS unsigned*)(L + L_CNT), 1u, __ATOMIC_RELAXED, __HIP_MEMORY_SCOPE_WORKGROUP);
      STAMP(6 + 2 * t);
      const float nb = __builtin_fmaf(rownorm(qf), cn, c0);
      f32x16 acc[2];
      SB();
      sfor<0, 84>([&](auto ic) {
        constexpr int n = decltype(ic)::v, u = n / 12, ks = n % 12;
        if constexpr (n < 72) {
          acc[u & 1] = mfma(A[n % 6], qf[ks], ks == 0 ? (f32x16){} : acc[u & 1]);
          if constexpr (n + 6 < 72) { constexpr int m = n + 6, uu = m / 12, kk = m % 12; A[n % 6] = ld128(L + L_K + ((2 + uu) % 7) * TILE + fa[kk & 3] + (kk >> 2) * 128); }
        }
        if constexpr (u >= 1 && ks >= 2 && ks < 10) {
          constexpr int i = ks - 2;
          const f32x16& pa = acc[(u - 1) & 1];
          pq[t][u - 1][i] = pk(__builtin_amdgcn_exp2f(__builtin_fmaf(pa[2 * i], c1, nb)), __builtin_amdgcn_exp2f(__builtin_fmaf(pa[2 * i + 1], c1, nb)));
          asm volatile("" : "+v"(pq[t][u - 1][i]));
        }
        SB();
      });
    }
    STAMP(3);
    CSTAMP(15);
    float inv[2];
    {
      for (unsigned it = 0; it < SPIN_MAX; ++it) {
        if (*(LAS volatile unsigned*)(L + L_KSC) == 3u) break;
        __builtin_amdgcn_s_sleep(1);
      }
      asm volatile("" ::: "memory");
      f32x16 d0 = {}, d1 = {};
#pragma unroll
      for (int us = 0; us < 12; ++us) {
        const u32x4 a = ld128(L + L_TAB + h * 16 + us * 32);
        const unsigned* p0 = &pq[0][us >> 1][(us & 1) * 4];
        const unsigned* p1 = &pq[1][us >> 1][(us & 1) * 4];
        d0 = mfma(a, (u32x4){p0[0], p0[1], p0[2], p0[3]}, d0);
        d1 = mfma(a, (u32x4){p1[0], p1[1], p1[2], p1[3]}, d1);
      }
      inv[0] = 1.0f / (d0[0] + 1e-6f); inv[1] = 1.0f / (d1[0] + 1e-6f);
      asm volatile("" : "+v"(inv[0]), "+v"(inv[1]));
    }
    STAMP(7);
    BAR();
    STAMP(4);
    const unsigned sa = w * 16384 + r32 * 128 + ((r32 & 7) << 4) + h * 8;
    {
      f32x16 acc[2][2];
      u32x4 A[6];
      const unsigned kb = L_V + l * 16;
      unsigned fr = 0;
#pragma unroll
      for (int i = 0; i < 6; ++i) A[i] = ld128(L + kb + i * 1024);
      SB();
      sfor<0, 82>([&](auto ic) {
        constexpr int n = decltype(ic)::v, c = n / 12, us = n % 12, pb = c & 1;
        if constexpr (n < 72) {
          const unsigned* p0 = &pq[0][us >> 1][(us & 1) * 4];
          const unsigned* p1 = &pq[1][us >> 1][(us & 1) * 4];
          acc[pb][0] = mfma(A[n % 6], (u32x4){p0[0], p0[1], p0[2], p0[3]}, us == 0 ? (f32x16){} : acc[pb][0]);
          acc[pb][1] = mfma(A[n % 6], (u32x4){p1[0], p1[1], p1[2], p1[3]}, us == 0 ? (f32x16){} : acc[pb][1]);
          if constexpr (n + 6 < 72) A[n % 6] = ld128(L + kb + (n + 6) * 1024);
        }
        if constexpr (n == 54) fr = *(LAS volatile unsigned*)(L + L_FR + w * 4);
        if constexpr (n == 60) {
          while (fr == 0u) fr = *(LAS volatile unsigned*)(L + L_FR + w * 4);
        }
        if constexpr (c >= 1 && us >= 1 && us <= 8) {
          constexpr int dt = c - 1, e = us - 1, t = e >> 2, qq = e & 3;
          const f32x16& a = acc[pb ^ 1][t];
          u32x2 o = {pk(a[4 * qq] * inv[t], a[4 * qq + 1] * inv[t]), pk(a[4 * qq + 2] * inv[t], a[4 * qq + 3] * inv[t])};
          *(LAS u32x2*)(L + (sa ^ ((((dt & 1) * 4 + qq)) << 4)) + ((dt >> 1) & 1) * 8192 + t * 4096) = o;
          if constexpr ((dt & 1) && us == 8) *(LAS volatile unsigned*)(L + L_SF + w * 4) = (dt >> 1) + 1;
        }
        SB();
      });
    }
    STAMP(5);
    __builtin_amdgcn_s_waitcnt(0);
    STAMP(9);
    DUMPTS();
    __builtin_amdgcn_s_waitcnt(0);
  } else {
    const int g = w - 4;
    char* const hello = ws + W_HEL + (size_t)head * 64;
    auto storer = [&]() {
      const __amdgpu_buffer_rsrc_t sO = __builtin_amdgcn_make_buffer_rsrc((void*)(out + hoff + (size_t)i8 * 256 * ROWB), 0, 256 * ROWB, 0x00020000);
      const unsigned sr = g * 16384 + (l >> 3) * 128 + (((l & 7) ^ ((l >> 3) & 7)) << 4);
      const unsigned so = (g * 64 + (l >> 3)) * ROWB + (l & 7) * 16;
#pragma unroll
      for (int cp = 0; cp < 3; ++cp) {
        for (unsigned it = 0; it < SPIN_MAX; ++it) {
          if (*(LAS volatile unsigned*)(L + L_SF + g * 4) > (unsigned)cp) break;
          __builtin_amdgcn_s_sleep(2);
        }
        asm volatile("" ::: "memory");
        u32x4 val[8];
#pragma unroll
        for (int i = 0; i < 8; ++i) val[i] = ld128(L + sr + (cp & 1) * 8192 + i * 1024);
        WAITL();
        if (cp == 0) *(LAS volatile unsigned*)(L + L_FR + g * 4) = 1u;
#pragma unroll
        for (int i = 0; i < 8; ++i) __builtin_amdgcn_raw_buffer_store_b128(val[i], sO, so + i * 8 * ROWB + cp * 128, 0, NT_OUT);
      }
      __builtin_amdgcn_s_waitcnt(0);
      STAMP(9);
    };
    if (w == 7) {
      unsigned vv[3];
#pragma unroll
      for (int p = 0; p < 3; ++p) { int Lc = p * 64 + l, row = Lc / 24, c = Lc % 24; vv[p] = row * ROWB + ((c ^ (((row >> 1) & 1) << 2)) << 4); }
      const __amdgpu_buffer_rsrc_t sV = __builtin_amdgcn_make_buffer_rsrc((void*)(v + hoff + (size_t)sc * 512 * ROWB), 0, 512 * ROWB, 0x00020000);
      auto issueV = [&](int j, int slot) {
#pragma unroll
        for (int a = 0; a < 4; ++a)
#pragma unroll
          for (int p = 0; p < 3; ++p) dma<NT_IN>(lbase + L_V + slot * TILE + a * 3072 + p * 1024, vv[p], sV, j * TILE + a * 3072);
      };
      const u64 old = ald(hello + i8 * 8);
      const u64 n = old * 0x9E3779B97F4A7C15ull + 0x632BE59BD9B4E019ull;
      if (l == 0) {
        ast(hello + i8 * 8, n);
        *(LAS u64*)(L + L_NON) = n; *(LAS unsigned*)(L + L_CNT) = 0u; *(LAS u64*)(L + L_FLG) = 0ull; *(LAS unsigned*)(L + L_KSC) = 0u;
        *(LAS u32x4*)(L + L_SF) = (u32x4){0u, 0u, 0u, 0u}; *(LAS u32x4*)(L + L_FR) = (u32x4){0u, 0u, 0u, 0u};
        *(LAS u32x4*)(L + L_Q0) = (u32x4){0u, 0u, 0u, 0u}; *(LAS unsigned*)(L + L_DRN) = 0u;
      }
      WAITV(0);
#pragma unroll 1
      for (int j = 0; j < 3; ++j) issueV(j, j);
      BAR();
      BAR();
      int islot = 2;
#pragma unroll 1
      for (int r = 0; r <= 9; ++r) {
        if (r >= 1) issueV(r + 2, islot);
        islot = islot == 4 ? 0 : islot + 1;
        RCLOSE(r, WAITV(36));
      }
      u32x4 q2[4][12];
      auto ldq2 = [&](auto jc) {
        constexpr int j = decltype(jc)::v;
#pragma unroll
        for (int a = 0; a < 4; ++a)
#pragma unroll
          for (int p = 0; p < 3; ++p)
            q2[j][3 * a + p] = __builtin_amdgcn_raw_buffer_load_b128(sQ, vo[p] ^ ((a & 1) << 6), (2 * j + 1) * TILE + a * 3072, Q2AUX);
      };
      ldq2(IC<0>{}); issueV(12, 2); RCLOSE(10, WAITV(48));
      ldq2(IC<1>{}); issueV(13, 3); RCLOSE(11, WAITV(60));
      ldq2(IC<2>{}); issueV(14, 4); RCLOSE(12, WAITV(60));
      ldq2(IC<3>{}); issueV(15, 0); RCLOSE(13, WAITV(60));
      issue12(IC<NT_Q>{}, lbase + L_V + 1 * TILE, sQ, 0 * TILE); RCLOSE(14, WAITV(60));
      issue12(IC<NT_Q>{}, lbase + L_V + 2 * TILE, sQ, 2 * TILE); RCLOSE(15, WAITV(48));
      issue12(IC<NT_Q>{}, lbase + L_V + 3 * TILE, sQ, 4 * TILE); RCLOSE(16, WAITV(36));
      issue12(IC<NT_Q>{}, lbase + L_V + 4 * TILE, sQ, 6 * TILE);
      DUMPACC();
      WAITV(36); if (l == 0) *(LAS volatile unsigned*)(L + L_FLG + 4) = 1u;
      WAITV(24); if (l == 0) *(LAS volatile unsigned*)(L + L_FLG + 4) = 2u;
      WAITV(12); if (l == 0) *(LAS volatile unsigned*)(L + L_FLG + 4) = 3u;
      WAITV(0);  if (l == 0) *(LAS volatile unsigned*)(L + L_FLG + 4) = 4u;
      sfor<0, 4>([&](auto jc) {
        constexpr int j = decltype(jc)::v;
        for (unsigned it = 0; it < SPIN_MAX; ++it) {
          if (*(LAS volatile unsigned*)(L + L_Q0 + j * 4) != 0u) break;
          __builtin_amdgcn_s_sleep(1);
        }
        asm volatile("" ::: "memory");
#pragma unroll
        for (int i = 0; i < 12; ++i) *(LAS u32x4*)(L + L_V + (1 + j) * TILE + i * 1024 + l * 16) = q2[j][i];
        WAITL();
        if (l == 0) *(LAS volatile unsigned*)(L + L_FLG + 4) = 5u + j;
      });
      BAR();
      storer();
    } else {
      BAR();
      BAR();
      const int blk = (l >> 4) & 1, q4 = (l & 15) >> 2, p4 = l & 3;
      unsigned xa[2], va[2];
#pragma unroll
      for (int t = 0; t < 2; ++t) xa[t] = L_X + (8 * h + 4 * t + q4) * 192 + ((((4 * blk + p4) ^ (4 * h + 2 * t + (q4 >> 1)))) << 3);
#pragma unroll
      for (int e = 0; e < 2; ++e) va[e] = L_V + (8 * h + q4) * ROWB + (2 * g + (e ^ ((q4 >> 1) & 1))) * 64 + blk * 32 + p4 * 8;
      f32x16 acc[2][3] = {};
      float ksum = 0.f, ksum2 = 0.f;
      u64 view = 0;
      u32x4 B[2][2], A[2][2][3];
      auto ldB = [&](unsigned vb) {
#pragma unroll
        for (int ks = 0; ks < 2; ++ks)
#pragma unroll
          for (int e = 0; e < 2; ++e) {
            const unsigned ad = va[e] + vb;
            u32x2 b0 = ldtr(L + ad + ks * 6144), b1 = ldtr(L + ad + ks * 6144 + 1536);
            B[ks][e] = (u32x4){b0[0], b0[1], b1[0], b1[1]};
          }
      };
      int vslot = 0;
      auto g2_round = [&](auto par, auto domul, auto doread) {
        constexpr int P = decltype(par)::v;
        constexpr bool M = decltype(domul)::v, R = decltype(doread)::v;
        SB();
        sfor<0, 12>([&](auto ic) {
          constexpr int n = decltype(ic)::v, ks = n / 6, j = (n % 6) >> 1, e = n & 1;
          if constexpr (M) acc[e][j] = mfma(A[P ^ 1][ks][j], B[ks][e], acc[e][j]);
          if constexpr (R && n < 6) {
            u32x2 a0 = ldtr(L + xa[0] + P * 6144 + (n / 3) * 3072 + (n % 3) * 64), a1 = ldtr(L + xa[1] + P * 6144 + (n / 3) * 3072 + (n % 3) * 64);
            A[P][n / 3][n % 3] = (u32x4){a0[0], a0[1], a1[0], a1[1]};
          }
          SB();
        });
        if constexpr (R) { ldB(vslot * TILE); vslot = vslot == 4 ? 0 : vslot + 1; }
        if constexpr (M) {
          auto sum = [&](auto jc) {
            constexpr int j = decltype(jc)::v;
#pragma unroll
            for (int ks = 0; ks < 2; ++ks)
#pragma unroll
              for (int i = 0; i < 4; ++i) {
                if (KSV) { ksum += bflo(A[P ^ 1][ks][j][i]); ksum2 += bfhi(A[P ^ 1][ks][j][i]); }
                else ksum = dot2(A[P ^ 1][ks][j][i], 0x3f803f80u, ksum);
              }
            asm volatile("" : "+v"(ksum), "+v"(ksum2));
          };
          if (g == 0) sum(IC<0>{}); else if (g == 1) sum(IC<1>{}); else sum(IC<2>{});
        }
      };
      const __amdgpu_buffer_rsrc_t sW = __builtin_amdgcn_make_buffer_rsrc((void*)ws, 0, (int)W_INB, 0x00020000);
      const unsigned p1own = (unsigned)W_P1 + (unsigned)((((head * 2 + mh) * 4 + sc) * 3 + g) * 12) * 1024u;
      auto finslot = [&](int pm, int f) {
        return (unsigned)W_FIN + (unsigned)(head * 72 + ((2 * g + f / 6) * 6 + 3 * pm + ((f % 6) >> 1)) * 2 + (f & 1)) * 1024u;
      };
      const unsigned ksown = (unsigned)W_KS + ((head * 8 + i8) * 3 + g) * 128;
      view = ald(hello + (l & 7) * 8);
      char* const inb = ws + W_INB;
      char* const myin = inb + ((size_t)(head * 8 + i8) * 3 + g) * 128;
      char* const tgt = inb + ((size_t)(head * 8 + (l & 7)) * 3 + g) * 128;
      const bool pub = l >= 8 && l < 16;
      if (G1PRIO) __builtin_amdgcn_s_setprio(G1PRIO);
      RCLOSE(0, );
      const u64 nown = *(LAS u64*)(L + L_NON);
      RCLOSE(1, );
      g2_round(IC<0>{}, IC<0>{}, IC<1>{});
      RCLOSE(2, );
#pragma unroll 1
      for (int r = 3; r <= 15; r += 2) {
        if (r <= 9) {
          const u32x4 e4 = {EMPTY, EMPTY, EMPTY, EMPTY};
          const int j0 = (r - 3) / 2 * 3;
#pragma unroll
          for (int i = 0; i < 3; ++i) {
            const unsigned a = r == 9 ? finslot(mh, 3 * sc + i) : p1own + (unsigned)(j0 < 3 * sc ? j0 + i : j0 + i + 3) * 1024u;
            __builtin_amdgcn_raw_buffer_store_b128(e4, sW, l * 16 + a, 0, XST);
          }
          if (r == 3 && l < 32) __builtin_amdgcn_raw_buffer_store_b32(EMPTY, sW, l * 4, ksown, XST);
        }
        if (r == PUBR) {
          WAITV(0);
          if (pub) ast(tgt + i8 * 8, view);
        }
        g2_round(IC<1>{}, IC<1>{}, IC<1>{});
        RCLOSE(r, );
        g2_round(IC<0>{}, IC<1>{}, IC<1>{});
        RCLOSE(r + 1, );
      }
      const char* const pa = pub ? hello + (l & 7) * 8 : myin + (l & 7) * 8;
      u64 x0 = ald(pa);
      g2_round(IC<1>{}, IC<1>{}, IC<1>{});
      g2_round(IC<0>{}, IC<1>{}, IC<0>{});
      DUMPACC();
      __builtin_amdgcn_s_setprio(XPRIO);
      STAMP(2);
      WAITL();
      if (l == 0) __hip_atomic_fetch_add((LAS unsigned*)(L + L_CNT), 1u, __ATOMIC_RELAXED, __HIP_MEMORY_SCOPE_WORKGROUP);
      float own[3][8];
#pragma unroll
      for (int f = 0; f < 12; ++f) {
        const f32x16& a = acc[f / 6][(f % 6) >> 1];
        const int o = (f & 1) * 8;
        if (f / 3 != sc) {
          u32x4 val = {ne(pk(a[o], a[o + 1])), ne(pk(a[o + 2], a[o + 3])), ne(pk(a[o + 4], a[o + 5])), ne(pk(a[o + 6], a[o + 7]))};
          __builtin_amdgcn_raw_buffer_store_b128(val, sW, l * 16 + p1own + f * 1024, 0, XST);
        } else {
#pragma unroll
          for (int i = 0; i < 8; ++i) own[f % 3][i] = a[o + i];
        }
      }
      {
        const float ks = halfsum(ksum + ksum2);
        if (l < 32) __builtin_amdgcn_raw_buffer_store_b32(ne(__float_as_uint(ks)), sW, l * 4, ksown, XST);
      }
      WAITV(0);
      if (DRAINW && l == 0) __hip_atomic_fetch_add((LAS unsigned*)(L + L_DRN), 1u, __ATOMIC_RELAXED, __HIP_MEMORY_SCOPE_WORKGROUP);
      STAMP(3);
      for (unsigned it = 0; it < SPIN_MAX; ++it) {
        if (pub && x0 != view) { view = x0; ast(tgt + i8 * 8, view); }
        if (__all(l >= 8 || x0 == nown)) break;
        __builtin_amdgcn_s_sleep(POLL_SLEEP);
        x0 = ald(pa);
      }
      asm volatile("" ::: "memory");
      const u64 xl = ald(hello + (l & 7) * 8);
      float kst;
      for (unsigned it = 0; it < SPIN_MAX; ++it) {
        asm volatile("" ::: "memory");
        unsigned kw[4];
#pragma unroll
        for (int s = 0; s < 4; ++s)
          kw[s] = __builtin_amdgcn_raw_buffer_load_b32(sW, h * 384 + r32 * 4, (unsigned)W_KS + ((head * 8 + 2 * s) * 3 + g) * 128, XLD);
        kst = (__uint_as_float(kw[0]) + __uint_as_float(kw[1])) + (__uint_as_float(kw[2]) + __uint_as_float(kw[3]));
        if (__all(max3u(max3u(kw[0], kw[1], kw[2]), kw[3], 0u) != EMPTY)) break;
        __builtin_amdgcn_s_sleep(POLL_SLEEP);
      }
      asm volatile("" ::: "memory");
      if (pub && xl != view) { view = xl; ast(tgt + i8 * 8, view); }
      STAMP(4);
      u32x4 pin[3][3];
      for (unsigned it = 0; it < SPIN_MAX; ++it) {
        asm volatile("" ::: "memory");
#pragma unroll
        for (int s = 0; s < 3; ++s) {
          const int so = s + (s >= sc);
          const unsigned base = (unsigned)W_P1 + (unsigned)(((((head * 2 + mh) * 4 + so) * 3 + g) * 12) + 3 * sc) * 1024u;
#pragma unroll
          for (int i = 0; i < 3; ++i) pin[s][i] = __builtin_amdgcn_raw_buffer_load_b128(sW, l * 16, base + i * 1024, XLD);
        }
        unsigned m = 0;
#pragma unroll
        for (int s = 0; s < 3; ++s)
#pragma unroll
          for (int i = 0; i < 3; ++i) m = max3u(max3u(m, pin[s][i][0], pin[s][i][1]), pin[s][i][2], pin[s][i][3]);
        if (__all(m != EMPTY)) break;
      }
      asm volatile("" ::: "memory");
      {
        const int u = 3 * h + g;
        const unsigned off = ((u * 2 + (r32 >> 4)) * 2 + ((r32 >> 2) & 1)) * 16 + (((r32 >> 3) & 1) * 4 + (r32 & 3)) * 2;
        *(LAS unsigned short*)(L + L_TAB + off) = (unsigned short)(pk(kst, 0.f) & 0xffffu);
        if (l == 0) __hip_atomic_fetch_add((LAS unsigned*)(L + L_KSC), 1u, __ATOMIC_RELAXED, __HIP_MEMORY_SCOPE_WORKGROUP);
      }
#pragma unroll
      for (int i = 0; i < 3; ++i) {
#pragma unroll
        for (int s = 0; s < 3; ++s)
#pragma unroll
          for (int e = 0; e < 4; ++e) { own[i][2 * e] += bflo(pin[s][i][e]); own[i][2 * e + 1] += bfhi(pin[s][i][e]); }
        u32x4 val = {ne(pk(own[i][0], own[i][1])), ne(pk(own[i][2], own[i][3])), ne(pk(own[i][4], own[i][5])), ne(pk(own[i][6], own[i][7]))};
        __builtin_amdgcn_raw_buffer_store_b128(val, sW, l * 16 + finslot(mh, 3 * sc + i), 0, XST);
      }
      STAMP(5);
      {
        const unsigned ha = finslot(l & 1, 3 * ((l >> 1) & 3) + 2) + 1020;
        for (unsigned it = 0; it < SPIN_MAX; ++it) {
          asm volatile("" ::: "memory");
          const unsigned x = __builtin_amdgcn_raw_buffer_load_b32(sW, ha, 0, XLD);
          if (__all(x != EMPTY)) break;
          __builtin_amdgcn_s_sleep(POLL_SLEEP);
        }
      }
      asm volatile("" ::: "memory");
      STAMP(6);
      u32x4 fin[24];
      const unsigned fb = (unsigned)W_FIN + (unsigned)(head * 72 + g * 24) * 1024u;
      for (unsigned it = 0; it < SPIN_MAX; ++it) {
        asm volatile("" ::: "memory");
#pragma unroll
        for (int i = 0; i < 24; ++i) fin[i] = __builtin_amdgcn_raw_buffer_load_b128(sW, l * 16, fb + i * 1024, XLD);
        unsigned m = 0;
#pragma unroll
        for (int i = 0; i < 24; ++i) m = max3u(max3u(m, fin[i][0], fin[i][1]), fin[i][2], fin[i][3]);
        if (__all(m != EMPTY)) break;
      }
      asm volatile("" ::: "memory");
      for (unsigned it = 0; it < SPIN_MAX; ++it) {
        if (__hip_atomic_load((LAS unsigned*)(L + L_CNT), __ATOMIC_RELAXED, __HIP_MEMORY_SCOPE_WORKGROUP) == 7u) break;
        __builtin_amdgcn_s_sleep(1);
      }
      asm volatile("" ::: "memory");
#pragma unroll
      for (int i = 0; i < 24; ++i) *(LAS u32x4*)(L + L_V + (g * 24 + i) * 1024 + l * 16) = fin[i];
      STAMP(7);
      __builtin_amdgcn_s_setprio(0);
      BAR();
      storer();
    }
    DUMPTS();
    __builtin_amdgcn_s_waitcnt(0);
  }
}

extern "C" void kernel_launch(void* const* d_in, const int* in_sizes, int n_in, void* d_out, int out_size, void* d_ws, size_t ws_size,
                              hipStream_t stream) {
  performer_fused<<<256, 512, 0, stream>>>((const char*)d_in[0], (const char*)d_in[1], (const char*)d_in[2], (const char*)d_in[3], (char*)d_out,
                                           (char*)d_ws);
}
```
